```python
import torch

def _softmax(x, axis=-1):
    return torch.softmax(x, dim=axis)
def _gelu(x):
    return torch.nn.functional.gelu(x, approximate="tanh")
def _silu(x):
    return torch.nn.functional.silu(x)
def _sigmoid(x):
    return torch.sigmoid(x)
def _layernorm(x, axis=-1, eps=1e-5):
    m = x.mean(dim=axis, keepdim=True)
    v = x.var(dim=axis, keepdim=True, unbiased=False)
    return (x - m) / torch.sqrt(v + eps)
def _scatter_add(tgt, idx, src, axis=0):
    return tgt.clone().index_add_(axis, idx, src)
def _segment_sum(src, seg, n):
    out = torch.zeros((n,) + src.shape[1:], dtype=src.dtype)
    return out.index_add_(0, seg, src)
def _pad(x, pw):
    flat = tuple(v for pair in reversed(pw) for v in pair)
    return torch.nn.functional.pad(x, flat)

kernel_name = 'parallel_sum__ssm_conv_s5__dense_activation_relu__prompt'

def setup_inputs():

    _w = (lambda a: (lambda t: t.to(torch.bfloat16) if t.is_floating_point() else t)(torch.as_tensor(a)))
    def _A_setup():
        B, S, D = 1, 2048, 512
        P = D
        Lambda_re = -0.5 * (lambda _x: torch.exp(torch.as_tensor(_x)))((lambda *s: torch.randn(*s, dtype=torch.bfloat16) * 0.8)(P))
        Lambda_im = (lambda *s: torch.randn(*s, dtype=torch.bfloat16) * 0.8)(P) * 1.0
        B_re = (lambda *s: torch.randn(*s, dtype=torch.bfloat16) * 0.8)(P, D) * (D ** -0.5)
        B_im = (lambda *s: torch.randn(*s, dtype=torch.bfloat16) * 0.8)(P, D) * (D ** -0.5)
        C_re = (lambda *s: torch.randn(*s, dtype=torch.bfloat16) * 0.8)(D, P) * (P ** -0.5)
        C_im = (lambda *s: torch.randn(*s, dtype=torch.bfloat16) * 0.8)(D, P) * (P ** -0.5)
        log_dt = (lambda *s: torch.randn(*s, dtype=torch.bfloat16) * 0.8)(P) * 0.3 - 2.0
        Dskip = (lambda *s: torch.randn(*s, dtype=torch.bfloat16) * 0.8)(D)
        return {
            "x": _w((lambda *s: torch.randn(*s, dtype=torch.bfloat16) * 0.05)(B, S, D)),
            "Lambda_re": _w(Lambda_re),
            "Lambda_im": _w(Lambda_im),
            "B_re": _w(B_re),
            "B_im": _w(B_im),
            "C_re": _w(C_re),
            "C_im": _w(C_im),
            "log_dt": _w(log_dt),
            "Dskip": _w(Dskip),
        }
    _A_in = _A_setup()

    def _B_setup():
        B, S, D, Dff = 1, 2048, 512, 1376
        return {
            "x": _w((lambda *s: torch.randn(*s, dtype=torch.bfloat16) * 0.05)(B, S, D)),
            "w1": _w((lambda *s: torch.randn(*s, dtype=torch.bfloat16) * 0.05)(D, Dff)),
            "w2": _w((lambda *s: torch.randn(*s, dtype=torch.bfloat16) * 0.05)(Dff, D)),
        }
    _B_in = _B_setup()

    return {
        "A_x": _A_in["x"],
        "A_Lambda_re": _A_in["Lambda_re"],
        "A_Lambda_im": _A_in["Lambda_im"],
        "A_B_re": _A_in["B_re"],
        "A_B_im": _A_in["B_im"],
        "A_C_re": _A_in["C_re"],
        "A_C_im": _A_in["C_im"],
        "A_log_dt": _A_in["log_dt"],
        "A_Dskip": _A_in["Dskip"],
        "B_x": _B_in["x"],
        "B_w1": _B_in["w1"],
        "B_w2": _B_in["w2"],
    }

def reference(A_x, A_Lambda_re, A_Lambda_im, A_B_re, A_B_im, A_C_re, A_C_im, A_log_dt, A_Dskip, B_x, B_w1, B_w2):
    def _A_ref(x, Lambda_re, Lambda_im, B_re, B_im, C_re, C_im, log_dt, Dskip):
        Bsz, L, H = x.shape
        Lam = Lambda_re + 1j * Lambda_im
        Bmat = B_re + 1j * B_im
        Cmat = C_re + 1j * C_im
        dt = torch.exp(log_dt)
        Lam_bar = torch.exp(dt * Lam)
        B_bar = ((Lam_bar - 1.0) / Lam)[:, None] * Bmat
        Bu = torch.einsum("bld,pd->blp", x + 0j, B_bar)
        h = torch.zeros_like(Bu[:, 0, :])
        hs = []
        for t in range(L):
            h = Lam_bar[None, :] * h + Bu[:, t, :]
            hs.append(h)
        hseq = torch.stack(hs, 1)
        y = 2.0 * torch.einsum("blp,dp->bld", hseq, Cmat).real + x * Dskip[None, None, :]
        return y

    def _B_ref(x, w1, w2):
        h = torch.einsum("bsd,df->bsf", x, w1)
        zero = torch.zeros_like(h)
        h = torch.maximum(h, zero)
        return torch.einsum("bsf,fd->bsd", h, w2)

    x = A_x
    xn = _layernorm(x, axis=-1)
    return x + _A_ref(xn, A_Lambda_re, A_Lambda_im, A_B_re, A_B_im, A_C_re, A_C_im, A_log_dt, A_Dskip) + _B_ref(xn, B_w1, B_w2)
```

```cpp
#include <hip/hip_runtime.h>
#include <hip/hip_bf16.h>
#include <stdint.h>

typedef __attribute__((ext_vector_type(8))) __bf16 bf8;
typedef __attribute__((ext_vector_type(4))) __bf16 bf4;
typedef __attribute__((ext_vector_type(2))) __bf16 bf2;
typedef __attribute__((ext_vector_type(4))) float f4;
typedef __attribute__((ext_vector_type(2))) float f2;
typedef __attribute__((ext_vector_type(4))) unsigned u4;
typedef __attribute__((ext_vector_type(2))) unsigned u2;
typedef unsigned long long u64;
typedef unsigned short u16;
#define LDS __attribute__((address_space(3)))
#define GLB __attribute__((address_space(1)))

constexpr int T = 2048, D = 512, P = 512, F = 1376;
constexpr int KH = 2432;
constexpr size_t WS_EPOCH = 0;
constexpr size_t WS_REC = 4096;
constexpr size_t WS_STAT = WS_REC + 16 * 8 * 2 * 64 * 8;
constexpr size_t WS_H = 1 << 20;

__device__ __forceinline__ void glds16(unsigned voff, const void* sbase, unsigned lds_addr) {
  unsigned keep;
  asm volatile("s_mov_b32 %0, m0\n\ts_mov_b32 m0, %3\n\ts_nop 0\n\tglobal_load_lds_dwordx4 %1, %2\n\ts_mov_b32 m0, %0"
               : "=&s"(keep) : "v"(voff), "s"(sbase), "s"(lds_addr));
}
#define WAIT_VM(n) asm volatile("s_waitcnt vmcnt(" #n ") lgkmcnt(0)" ::: "memory")
#define BARRIER() do { __builtin_amdgcn_s_barrier(); asm volatile("" ::: "memory"); } while (0)
#define LDS_SYNC() do { asm volatile("s_waitcnt lgkmcnt(0)" ::: "memory"); BARRIER(); } while (0)

__device__ __forceinline__ bf8 lds_b128(unsigned a) { return *(LDS const bf8*)(size_t)a; }
__device__ __forceinline__ bf8 lds_tr(unsigned a) {
  bf4 lo = __builtin_amdgcn_ds_read_tr16_b64_v4bf16((LDS bf4*)(size_t)a);
  bf4 hi = __builtin_amdgcn_ds_read_tr16_b64_v4bf16((LDS bf4*)(size_t)(a + 512));
  return __builtin_shufflevector(lo, hi, 0, 1, 2, 3, 4, 5, 6, 7);
}
__device__ __forceinline__ f4 mfma(bf8 a, bf8 b, f4 c) { return __builtin_amdgcn_mfma_f32_16x16x32_bf16(a, b, c, 0, 0, 0); }
__device__ __forceinline__ float dot_ones(bf8 v, float c) {
  const bf2 one = {(__bf16)1.0f, (__bf16)1.0f};
#pragma unroll
  for (int i = 0; i < 4; ++i) c = __builtin_amdgcn_fdot2_f32_bf16((bf2){v[2 * i], v[2 * i + 1]}, one, c, false);
  return c;
}
__device__ __forceinline__ float dot_self(bf8 v, float c) {
#pragma unroll
  for (int i = 0; i < 4; ++i) { bf2 p = {v[2 * i], v[2 * i + 1]}; c = __builtin_amdgcn_fdot2_f32_bf16(p, p, c, false); }
  return c;
}
__device__ __forceinline__ float bfr(float v) { return (float)(__bf16)v; }
__device__ __forceinline__ float bf2f(u16 v) { return __uint_as_float((unsigned)v << 16); }
__device__ __forceinline__ unsigned pack2(float a, float b) { bf2 p = {(__bf16)a, (__bf16)b}; return __builtin_bit_cast(unsigned, p); }
__device__ __forceinline__ float sum_g(float v) { v += __shfl_xor(v, 16); v += __shfl_xor(v, 32); return v; }
__device__ __forceinline__ void lds_st(unsigned a, float v) { *(LDS float*)(size_t)a = v; }
__device__ __forceinline__ float lds_ld(unsigned a) { return *(LDS const float*)(size_t)a; }
__device__ __forceinline__ f4 lds_ld4(unsigned a) { return *(LDS const f4*)(size_t)a; }
__device__ __forceinline__ f2 lds_ld2(unsigned a) { return *(LDS const f2*)(size_t)a; }
__device__ __forceinline__ void lds_st2(unsigned a, f2 v) { *(LDS f2*)(size_t)a = v; }
__device__ __forceinline__ void lds_st4(unsigned a, f4 v) { *(LDS f4*)(size_t)a = v; }
__device__ __forceinline__ f2 cmul_add(f2 a, f2 b, f2 c) {
  return (f2){fmaf(-a.y, b.y, fmaf(a.x, b.x, c.x)), fmaf(a.y, b.x, fmaf(a.x, b.y, c.y))};
}
__device__ __forceinline__ f2 cmul(f2 a, f2 b) { return (f2){a.x * b.x - a.y * b.y, a.x * b.y + a.y * b.x}; }

struct Params {
  const u16* x; const u16* lre; const u16* lim; const u16* bre; const u16* bim; const u16* cre; const u16* cim;
  const u16* ldt; const u16* dsk; const u16* w1; const u16* w2; float* out; char* ws;
};

constexpr unsigned E_PART = 0;
constexpr unsigned E_MEAN = 4096;
constexpr unsigned E_RINV = 4608;
constexpr unsigned E_CS = 5120;
constexpr unsigned E_AGG = 8192;
constexpr unsigned E_CIN = 12288;
constexpr unsigned K1_CONST = 131072;
constexpr unsigned K1_LDS = K1_CONST + 1024;

template <bool SSM>
__device__ __forceinline__ void k1_body(const Params& p, int rt, int j, int kst) {
  constexpr int NT = SSM ? 2 : 3;
  constexpr int NSLOT = SSM ? 4 : 3;
  constexpr unsigned SLOT = SSM ? 32768 : 40960;
  constexpr int NG = SSM ? 4 : 5;
  const int tid = threadIdx.x, lane = tid & 63;
  const int w = __builtin_amdgcn_readfirstlane(tid >> 6), m = w >> 2, n = w & 3;
  const int c = lane & 15, g = lane >> 4;
  const int t0 = rt * 128;
  int f_lo = 0, f_hi = 0, f_ld = 0;
  if (!SSM) { f_lo = 16 * ((86 * j) >> 3); f_hi = 16 * ((86 * (j + 1)) >> 3); f_ld = f_lo < F - 192 ? f_lo : F - 192; }

  unsigned voA[2], voB[3];
#pragma unroll
  for (int u = 0; u < 2; ++u) {
    const int row = 8 * (2 * w + u) + (lane >> 3), key = (row & 3) | ((row >> 2) & 4);
    voA[u] = (unsigned)(row * D + (((lane & 7) ^ key) << 3)) * 2;
  }
  const char* baseA = (const char*)(p.x + (size_t)t0 * D);
  const char* baseB;
  if (SSM) {
#pragma unroll
    for (int u = 0; u < 2; ++u) {
      const int row = (8 * (2 * w + u) + (lane >> 3)) & 63;
      voB[u] = (unsigned)(row * D + (((lane & 7) ^ (row & 7)) << 3)) * 2;
    }
    baseB = (const char*)((w < 4 ? p.bre : p.bim) + (size_t)j * 64 * D);
  } else {
#pragma unroll
    for (int u = 0; u < 3; ++u) {
      const int i = 3 * w + u, k = 8 * (i & 7) + (lane >> 3), key = ((k >> 1) & 1) | ((k >> 2) & 2);
      voB[u] = (unsigned)(k * F + 64 * (i >> 3) + (((lane & 7) ^ (key << 1)) << 3)) * 2;
    }
    baseB = (const char*)(p.w1 + f_ld);
  }
  auto stage = [&](int s) {
    const int ks = (s + kst) & 7;
    const unsigned slot = (unsigned)(s % NSLOT) * SLOT;
    const char* a = baseA + ks * 128;
#pragma unroll
    for (int u = 0; u < 2; ++u) glds16(voA[u], a, slot + (2 * w + u) * 1024);
    if (SSM) {
      const char* b = baseB + ks * 128;
#pragma unroll
      for (int u = 0; u < 2; ++u) glds16(voB[u], b, slot + 16384 + (2 * w + u) * 1024);
    } else {
      const char* b = baseB + (size_t)ks * 64 * F * 2;
#pragma unroll
      for (int u = 0; u < 3; ++u) glds16(voB[u], b, slot + 16384 + (3 * w + u) * 1024);
    }
  };
  unsigned r_lre = 0, r_lim = 0, r_ldt = 0, tag = 0;
  if (SSM && w == 0) {
    const unsigned o = (unsigned)(j * 64 + lane) * 2, z = 0;
    asm volatile("global_load_ushort %0, %4, %6\n\tglobal_load_ushort %1, %4, %7\n\tglobal_load_ushort %2, %4, %8\n\t"
                 "global_load_dword %3, %5, %9 sc1"
                 : "=&v"(r_lre), "=&v"(r_lim), "=&v"(r_ldt), "=&v"(tag)
                 : "v"(o), "v"(z), "s"(p.lre), "s"(p.lim), "s"(p.ldt), "s"(p.ws + WS_EPOCH + j * 128));
  }
#pragma unroll
  for (int s = 0; s < NSLOT - 1; ++s) stage(s);

  unsigned adA[2], adB[3];
  {
    const int row = 64 * m + 16 * (c >> 2) + (c & 3);
    adA[0] = row * 128 + ((g ^ (c & 7)) << 4);
    adA[1] = adA[0] ^ 64;
    if (SSM) {
      adB[0] = 16384 + (16 * n + c) * 128 + ((g ^ (c & 7)) << 4);
      adB[1] = adB[0] ^ 64;
    } else {
      const int q = c >> 2, pp = c & 3, key = ((q >> 1) & 1) | ((g & 1) << 1);
#pragma unroll
      for (int u = 0; u < 3; ++u) {
        const int ct = 3 * n + u;
        adB[u] = 16384 + (ct >> 2) * 8192 + (8 * g + q) * 128 + (((((ct & 3) ^ key) << 1) | (pp >> 1)) << 4) + 8 * (pp & 1);
      }
    }
  }

  f4 acc[4][NT];
#pragma unroll
  for (int a = 0; a < 4; ++a)
#pragma unroll
    for (int u = 0; u < NT; ++u) acc[a][u] = (f4){0.f, 0.f, 0.f, 0.f};
  float rs[4] = {0.f, 0.f, 0.f, 0.f}, rq[4] = {0.f, 0.f, 0.f, 0.f}, cs[NT];
#pragma unroll
  for (int u = 0; u < NT; ++u) cs[u] = 0.f;

#pragma unroll
  for (int s = 0; s < 8; ++s) {
    if (SSM) { if (s + 2 < 8) WAIT_VM(8); else if (s + 1 < 8) WAIT_VM(4); else WAIT_VM(0); }
    else     { if (s + 1 < 8) WAIT_VM(5); else WAIT_VM(0); }
    BARRIER();
    if (s + NSLOT - 1 < 8) stage(s + NSLOT - 1);
    if (SSM && s == 0 && w == 0) {
      asm volatile("" : "+v"(r_lre), "+v"(r_lim), "+v"(r_ldt), "+v"(tag));
      tag += 1u;
      const float lr = bf2f((u16)r_lre), li = bf2f((u16)r_lim);
      const float dt = bfr(expf(bf2f((u16)r_ldt)));
      const float e = expf(dt * lr);
      float sn, cn; sincosf(dt * li, &sn, &cn);
      const f2 lam = {e * cn, e * sn};
      const float nr = lam.x - 1.0f, ni = lam.y, den = lr * lr + li * li;
      lds_st2(K1_CONST + lane * 8, lam);
      lds_st2(K1_CONST + 512 + lane * 8, (f2){(nr * lr + ni * li) / den, (ni * lr - nr * li) / den});
    }
    const unsigned slot = (unsigned)(s % NSLOT) * SLOT;
#pragma unroll
    for (int ks = 0; ks < 2; ++ks) {
      bf8 A[4], B[NT];
#pragma unroll
      for (int a = 0; a < 4; ++a) A[a] = lds_b128(slot + adA[ks] + 512 * a);
      if (SSM) {
        B[0] = lds_b128(slot + adB[ks]);
        B[1] = lds_b128(slot + adB[ks] + 8192);
      } else {
#pragma unroll
        for (int u = 0; u < 3; ++u) B[u] = lds_tr(slot + adB[u] + ks * 4096);
      }
#pragma unroll
      for (int a = 0; a < 4; ++a)
#pragma unroll
        for (int u = 0; u < NT; ++u) acc[a][u] = mfma(A[a], B[u], acc[a][u]);
      if (((2 * s + ks + 2 * m) & 3) == n) {
#pragma unroll
        for (int a = 0; a < 4; ++a) { rs[a] = dot_ones(A[a], rs[a]); rq[a] = dot_self(A[a], rq[a]); }
      }
      if (ks == m) {
#pragma unroll
        for (int u = 0; u < NT; ++u) cs[u] = dot_ones(B[u], cs[u]);
      }
    }
  }
  LDS_SYNC();

#pragma unroll
  for (int a = 0; a < 4; ++a) { rs[a] = sum_g(rs[a]); rq[a] = sum_g(rq[a]); }
#pragma unroll
  for (int u = 0; u < NT; ++u) cs[u] = sum_g(cs[u]);
  if (g == 0) {
#pragma unroll
    for (int a = 0; a < 4; ++a) {
      const int row = 64 * m + 16 * (c >> 2) + 4 * a + (c & 3);
      lds_st2(E_PART + (n * 128 + row) * 8, (f2){rs[a], rq[a]});
    }
#pragma unroll
    for (int u = 0; u < NT; ++u) lds_st(E_CS + ((m * 12 + n * NT + u) * 16 + c) * 4, cs[u]);
  }
  LDS_SYNC();
  if (tid < 128) {
    f2 s = lds_ld2(E_PART + tid * 8);
#pragma unroll
    for (int i = 1; i < 4; ++i) s += lds_ld2(E_PART + (i * 128 + tid) * 8);
    const float mean = s.x * (1.0f / D);
    const float var = fmaxf(s.y * (1.0f / D) - mean * mean, 0.f);
    const float mb = bfr(mean), den = bfr(sqrtf(bfr(bfr(var) + 1e-5f)));
    lds_st(E_MEAN + tid * 4, mb);
    lds_st(E_RINV + tid * 4, 1.0f / den);
    if (SSM && j == 0) ((f2*)(p.ws + WS_STAT))[t0 + tid] = (f2){mb, den};
  }
#pragma unroll
  for (int u = 0; u < NT; ++u)
    cs[u] = lds_ld(E_CS + ((n * NT + u) * 16 + c) * 4) + lds_ld(E_CS + ((12 + n * NT + u) * 16 + c) * 4);
  LDS_SYNC();
  f4 mean4[4], rinv4[4];
#pragma unroll
  for (int a = 0; a < 4; ++a) {
    mean4[a] = lds_ld4(E_MEAN + (64 * m + 16 * g + 4 * a) * 4);
    rinv4[a] = lds_ld4(E_RINV + (64 * m + 16 * g + 4 * a) * 4);
  }
#pragma unroll
  for (int a = 0; a < 4; ++a)
#pragma unroll
    for (int u = 0; u < NT; ++u)
#pragma unroll
      for (int i = 0; i < 4; ++i) acc[a][u][i] = (acc[a][u][i] - mean4[a][i] * cs[u]) * rinv4[a][i];

  char* ht = p.ws + WS_H + (size_t)(2 * rt + m) * KH * 128 + g * 32;
  if (!SSM) {
#pragma unroll
    for (int u = 0; u < 3; ++u) {
      const int f = f_ld + 16 * (3 * n + u);
      if (f >= f_lo && f < f_hi) {
        u4 o[2];
#pragma unroll
        for (int a = 0; a < 4; ++a) {
          o[a >> 1][2 * (a & 1)] = pack2(fmaxf(acc[a][u][0], 0.f), fmaxf(acc[a][u][1], 0.f));
          o[a >> 1][2 * (a & 1) + 1] = pack2(fmaxf(acc[a][u][2], 0.f), fmaxf(acc[a][u][3], 0.f));
        }
        u4* dst = (u4*)(ht + (size_t)(1024 + f + c) * 128);
        dst[0] = o[0]; dst[1] = o[1];
      }
    }
  } else {
    const int chl = 16 * n + c, seg = 4 * m + g;
    const f2 lam = lds_ld2(K1_CONST + chl * 8), cc = lds_ld2(K1_CONST + 512 + chl * 8);
    f2 st = {0.f, 0.f};
#pragma unroll
    for (int a = 0; a < 4; ++a)
#pragma unroll
      for (int i = 0; i < 4; ++i) {
        const f2 bu = cmul(cc, (f2){acc[a][0][i], acc[a][1][i]});
        acc[a][0][i] = bu.x; acc[a][1][i] = bu.y;
        st = cmul_add(lam, st, bu);
      }
    lds_st2(E_AGG + (seg * 64 + chl) * 8, st);
    LDS_SYNC();
    if (w == 0) {
      const f2 l1 = lds_ld2(K1_CONST + lane * 8);
      f2 l16 = l1;
#pragma unroll
      for (int i = 0; i < 4; ++i) l16 = cmul(l16, l16);
      f2 l128 = l16;
#pragma unroll
      for (int i = 0; i < 3; ++i) l128 = cmul(l128, l128);
      f2 ag[8];
#pragma unroll
      for (int i = 0; i < 8; ++i) ag[i] = lds_ld2(E_AGG + (i * 64 + lane) * 8);
      f2 tot = ag[0];
#pragma unroll
      for (int i = 1; i < 8; ++i) tot = cmul_add(l16, tot, ag[i]);
      GLB u64* rec = (GLB u64*)(p.ws + WS_REC) + (size_t)j * 128 + lane;
      if (rt < 15) {
        __hip_atomic_store(rec + rt * 1024, ((u64)tag << 32) | __float_as_uint(tot.x), __ATOMIC_RELAXED, __HIP_MEMORY_SCOPE_AGENT);
        __hip_atomic_store(rec + rt * 1024 + 64, ((u64)tag << 32) | __float_as_uint(tot.y), __ATOMIC_RELAXED, __HIP_MEMORY_SCOPE_AGENT);
      }
      f2 carry = {0.f, 0.f};
      if (rt > 0) {
        u64 vr[15], vi[15];
        for (int spin = 0; spin < 400000; ++spin) {
          bool ok = true;
#pragma unroll
          for (int i = 0; i < 15; ++i) {
            const int ii = i < rt ? i : 0;
            vr[i] = __hip_atomic_load(rec + ii * 1024, __ATOMIC_RELAXED, __HIP_MEMORY_SCOPE_AGENT);
            vi[i] = __hip_atomic_load(rec + ii * 1024 + 64, __ATOMIC_RELAXED, __HIP_MEMORY_SCOPE_AGENT);
          }
#pragma unroll
          for (int i = 0; i < 15; ++i) ok = ok && (unsigned)(vr[i] >> 32) == tag && (unsigned)(vi[i] >> 32) == tag;
          if (__all(ok)) break;
          __builtin_amdgcn_s_sleep(2);
        }
#pragma unroll
        for (int i = 0; i < 15; ++i)
          if (i < rt) carry = cmul_add(l128, carry, (f2){__uint_as_float((unsigned)vr[i]), __uint_as_float((unsigned)vi[i])});
        if (rt == 15 && lane == 0)
          __hip_atomic_store((GLB unsigned*)(p.ws + WS_EPOCH) + j * 32, tag, __ATOMIC_RELAXED, __HIP_MEMORY_SCOPE_AGENT);
      }
#pragma unroll
      for (int i = 0; i < 8; ++i) {
        lds_st2(E_CIN + (i * 64 + lane) * 8, carry);
        carry = cmul_add(l16, carry, ag[i]);
      }
    }
    LDS_SYNC();
    f2 h = lds_ld2(E_CIN + (seg * 64 + chl) * 8);
    u4 ore[2], oim[2];
#pragma unroll
    for (int a = 0; a < 4; ++a) {
      float hr[4], hi[4];
#pragma unroll
      for (int i = 0; i < 4; ++i) {
        h = cmul_add(lam, h, (f2){acc[a][0][i], acc[a][1][i]});
        hr[i] = 2.0f * h.x; hi[i] = -2.0f * h.y;
      }
      ore[a >> 1][2 * (a & 1)] = pack2(hr[0], hr[1]); ore[a >> 1][2 * (a & 1) + 1] = pack2(hr[2], hr[3]);
      oim[a >> 1][2 * (a & 1)] = pack2(hi[0], hi[1]); oim[a >> 1][2 * (a & 1) + 1] = pack2(hi[2], hi[3]);
    }
    u4* dre = (u4*)(ht + (size_t)(j * 64 + chl) * 128);
    u4* dim = (u4*)(ht + (size_t)(512 + j * 64 + chl) * 128);
    dre[0] = ore[0]; dre[1] = ore[1]; dim[0] = oim[0]; dim[1] = oim[1];
  }
}

__global__ __launch_bounds__(512, 2) void s5_k1(Params p) {
  const int b = blockIdx.x, xcd = b & 7, idx = b >> 3;
  const int rt = 2 * xcd + (idx >> 4), j = idx & 15;
  const int kst = (idx + (idx >> 4) * 4) & 7;
  if (j < 8) k1_body<true>(p, rt, j, kst);
  else k1_body<false>(p, rt, j - 8, kst);
}

constexpr unsigned K2_LDS = 131072;
__global__ __launch_bounds__(256, 1) void s5_k2(Params p) {
  const int b = blockIdx.x, xcd = b & 7, idx = b >> 3;
  const int tt = 4 * xcd + (idx >> 3), dt = idx & 7;
  const int tid = threadIdx.x, lane = tid & 63;
  const int w = __builtin_amdgcn_readfirstlane(tid >> 6);
  const int c = lane & 15, g = lane >> 4;
  const int d0 = dt * 64, t0 = tt * 64;
  const int s0 = (idx * 5) % 19;

  unsigned voH[4], voN[4], voK[4];
#pragma unroll
  for (int u = 0; u < 4; ++u) {
    const int i = 4 * w + u, r8 = lane >> 3, pos = lane & 7;
    const int k = 8 * i + r8, key = ((k >> 1) & 1) | ((k >> 2) & 2);
    const int chunk = pos ^ (key << 1);
    voH[u] = (unsigned)(k * 128 + chunk * 16);
    voK[u] = (unsigned)(k * (D * 2) + chunk * 16);
    const int row = 8 * (i & 7) + r8;
    voN[u] = (unsigned)(row * (P * 2) + (i >> 3) * 128 + ((pos ^ (row & 7)) << 4));
  }
  const char* hbase = p.ws + WS_H + (size_t)tt * KH * 128;
  auto stage = [&](int s) {
    int st = s + s0; st = st >= 19 ? st - 19 : st;
    const unsigned slot = (unsigned)(s & 3) * 32768;
    const char* hb = hbase + (size_t)st * 16384;
    if (st < 8) {
      const char* wb = (const char*)((st < 4 ? p.cre : p.cim) + (size_t)d0 * P + (st & 3) * 128);
#pragma unroll
      for (int u = 0; u < 4; ++u) glds16(voN[u], wb, slot + (4 * w + u) * 1024);
    } else {
      const char* wb = (const char*)(p.w2 + (size_t)(st - 8) * 128 * D + d0);
      if (st == 18 && w == 3) wb -= 32 * D * 2;
#pragma unroll
      for (int u = 0; u < 4; ++u) glds16(voK[u], wb, slot + (4 * w + u) * 1024);
    }
#pragma unroll
    for (int u = 0; u < 4; ++u) glds16(voH[u], hb, slot + 16384 + (4 * w + u) * 1024);
  };
  u2 xr[4], sr[4], dr;
  {
    const unsigned ox = (unsigned)((16 * w + g) * D + d0 + 4 * c) * 2, os = (unsigned)(16 * w + g) * 8, od = (unsigned)(d0 + 4 * c) * 2;
    const char* xb = (const char*)(p.x + (size_t)t0 * D);
    const char* sb = p.ws + WS_STAT + (size_t)t0 * 8;
    asm volatile("global_load_dwordx2 %0, %6, %9\n\tglobal_load_dwordx2 %1, %7, %10\n\t"
                 "global_load_dwordx2 %2, %7, %10 offset:32\n\tglobal_load_dwordx2 %3, %7, %10 offset:64\n\t"
                 "global_load_dwordx2 %4, %7, %10 offset:96\n\tglobal_load_dwordx2 %5, %8, %11"
                 : "=&v"(xr[0]), "=&v"(sr[0]), "=&v"(sr[1]), "=&v"(sr[2]), "=&v"(sr[3]), "=&v"(dr)
                 : "v"(ox), "v"(os), "v"(od), "s"(xb), "s"(sb), "s"(p.dsk));
    asm volatile("global_load_dwordx2 %0, %3, %4\n\tglobal_load_dwordx2 %1, %3, %5\n\tglobal_load_dwordx2 %2, %3, %6"
                 : "=&v"(xr[1]), "=&v"(xr[2]), "=&v"(xr[3])
                 : "v"(ox), "s"(xb + 4 * D * 2), "s"(xb + 8 * D * 2), "s"(xb + 12 * D * 2));
  }
  stage(0); stage(1); stage(2);

  unsigned adT[4], adN;
  {
    const int q = c >> 2, pp = c & 3, key = ((q >> 1) & 1) | ((g & 1) << 1);
#pragma unroll
    for (int u = 0; u < 4; ++u) adT[u] = (32 * w + 8 * g + q) * 128 + ((((u ^ key) << 1) | (pp >> 1)) << 4) + 8 * (pp & 1);
    adN = (w >> 1) * 8192 + c * 128 + (((4 * (w & 1) + g) ^ (c & 7)) << 4);
  }
  f4 acc[4][4];
#pragma unroll
  for (int a = 0; a < 4; ++a)
#pragma unroll
    for (int u = 0; u < 4; ++u) acc[a][u] = (f4){0.f, 0.f, 0.f, 0.f};

  for (int s = 0; s < 19; ++s) {
    if (s < 17) WAIT_VM(16); else if (s == 17) WAIT_VM(8); else WAIT_VM(0);
    BARRIER();
    if (s + 3 < 19) stage(s + 3);
    int st = s + s0; st = st >= 19 ? st - 19 : st;
    const unsigned slot = (unsigned)(s & 3) * 32768;
    if (st == 18 && w == 3) continue;
    bf8 W[4], H[4];
    if (st < 8) {
#pragma unroll
      for (int a = 0; a < 4; ++a) W[a] = lds_b128(slot + adN + a * 2048);
    } else {
#pragma unroll
      for (int a = 0; a < 4; ++a) W[a] = lds_tr(slot + adT[a]);
    }
#pragma unroll
    for (int u = 0; u < 4; ++u) H[u] = lds_tr(slot + 16384 + adT[u]);
#pragma unroll
    for (int a = 0; a < 4; ++a)
#pragma unroll
      for (int u = 0; u < 4; ++u) acc[a][u] = mfma(W[a], H[u], acc[a][u]);
  }
  LDS_SYNC();
#pragma unroll
  for (int a = 0; a < 4; ++a)
#pragma unroll
    for (int u = 0; u < 4; ++u) {
      const int t = 16 * u + c;
      lds_st4(w * 16384 + t * 256 + (((4 * a + g) ^ (t & 7)) << 4), acc[a][u]);
    }
  LDS_SYNC();
  asm volatile("" : "+v"(xr[0]), "+v"(xr[1]), "+v"(xr[2]), "+v"(xr[3]), "+v"(sr[0]), "+v"(sr[1]), "+v"(sr[2]), "+v"(sr[3]), "+v"(dr));
  const f4 dsk = {__uint_as_float(dr.x << 16), __uint_as_float(dr.x & 0xffff0000u), __uint_as_float(dr.y << 16), __uint_as_float(dr.y & 0xffff0000u)};
#pragma unroll
  for (int r = 0; r < 4; ++r) {
    const int t = 16 * w + 4 * r + g;
    const unsigned a = t * 256 + ((c ^ (t & 7)) << 4);
    f4 v = lds_ld4(a) + lds_ld4(a + 16384) + lds_ld4(a + 32768) + lds_ld4(a + 49152);
    const f2 stt = {__uint_as_float(sr[r].x), __uint_as_float(sr[r].y)};
    const float xv[4] = {__uint_as_float(xr[r].x << 16), __uint_as_float(xr[r].x & 0xffff0000u),
                         __uint_as_float(xr[r].y << 16), __uint_as_float(xr[r].y & 0xffff0000u)};
#pragma unroll
    for (int i = 0; i < 4; ++i) {
      const float xn = bfr(bfr(xv[i] - stt.x) / stt.y);
      v[i] = xv[i] + (v[i] + bfr(xn * dsk[i]));
    }
    *(f4*)(p.out + (size_t)(t0 + t) * D + d0 + 4 * c) = v;
  }
}

extern "C" void kernel_launch(void* const* d_in, const int* in_sizes, int n_in, void* d_out, int out_size,
                              void* d_ws, size_t ws_size, hipStream_t stream) {
  Params p;
  p.x = (const u16*)d_in[0]; p.lre = (const u16*)d_in[1]; p.lim = (const u16*)d_in[2];
  p.bre = (const u16*)d_in[3]; p.bim = (const u16*)d_in[4]; p.cre = (const u16*)d_in[5]; p.cim = (const u16*)d_in[6];
  p.ldt = (const u16*)d_in[7]; p.dsk = (const u16*)d_in[8]; p.w1 = (const u16*)d_in[10]; p.w2 = (const u16*)d_in[11];
  p.out = (float*)d_out; p.ws = (char*)d_ws;
  (void)hipFuncSetAttribute((const void*)s5_k1, hipFuncAttributeMaxDynamicSharedMemorySize, K1_LDS);
  (void)hipFuncSetAttribute((const void*)s5_k2, hipFuncAttributeMaxDynamicSharedMemorySize, K2_LDS);
  s5_k1<<<256, 512, K1_LDS, stream>>>(p);
  s5_k2<<<256, 256, K2_LDS, stream>>>(p);
}
```

```cpp
#include <hip/hip_runtime.h>
#include <hip/hip_bf16.h>
#include <stdint.h>

typedef __attribute__((ext_vector_type(8))) __bf16 bf8;
typedef __attribute__((ext_vector_type(4))) __bf16 bf4;
typedef __attribute__((ext_vector_type(2))) __bf16 bf2;
typedef __attribute__((ext_vector_type(4))) float f4;
typedef __attribute__((ext_vector_type(2))) float f2;
typedef __attribute__((ext_vector_type(4))) unsigned u4;
typedef __attribute__((ext_vector_type(2))) unsigned u2;
typedef unsigned long long u64;
typedef unsigned short u16;
#define LDS __attribute__((address_space(3)))
#define GLB __attribute__((address_space(1)))

constexpr int T = 2048, D = 512, P = 512, F = 1376;
constexpr int KH = 2432;
constexpr size_t WS_EPOCH = 0;
constexpr size_t WS_REC = 4096;
constexpr size_t WS_STAT = WS_REC + 16 * 8 * 2 * 64 * 8;
constexpr size_t WS_STAMP = 128u << 20;
#define STAMP_DECL
#define STAMP(i)
#define STAMP_OUT(k)
constexpr size_t WS_H = 1 << 20;

__device__ __forceinline__ void glds16(unsigned voff, const void* sbase, unsigned lds_addr) {
  unsigned keep;
  asm volatile("s_mov_b32 %0, m0\n\ts_mov_b32 m0, %3\n\ts_nop 0\n\tglobal_load_lds_dwordx4 %1, %2\n\ts_mov_b32 m0, %0"
               : "=&s"(keep) : "v"(voff), "s"(sbase), "s"(lds_addr));
}
__device__ __forceinline__ void glds_x4(const unsigned* vo, const char* sbase, unsigned lds_addr) {
  unsigned keep;
  asm volatile("s_mov_b32 %0, m0\n\ts_mov_b32 m0, %6\n\ts_nop 0\n\tglobal_load_lds_dwordx4 %1, %5\n\t"
               "global_load_lds_dwordx4 %2, %5 offset:1024\n\tglobal_load_lds_dwordx4 %3, %5 offset:2048\n\t"
               "global_load_lds_dwordx4 %4, %5 offset:3072\n\ts_mov_b32 m0, %0"
               : "=&s"(keep) : "v"(vo[0]), "v"(vo[1]), "v"(vo[2]), "v"(vo[3]), "s"(sbase - 4096), "s"(lds_addr));
}
__device__ __forceinline__ void glds_x2(const unsigned* vo, const char* sbase, unsigned lds_addr) {
  unsigned keep;
  asm volatile("s_mov_b32 %0, m0\n\ts_mov_b32 m0, %4\n\ts_nop 0\n\tglobal_load_lds_dwordx4 %1, %3\n\t"
               "global_load_lds_dwordx4 %2, %3 offset:1024\n\ts_mov_b32 m0, %0"
               : "=&s"(keep) : "v"(vo[0]), "v"(vo[1]), "s"(sbase - 4096), "s"(lds_addr));
}
#define WAIT_VM(n) asm volatile("s_waitcnt vmcnt(" #n ") lgkmcnt(0)" ::: "memory")
#define BARRIER() do { __builtin_amdgcn_s_barrier(); asm volatile("" ::: "memory"); } while (0)
#define LDS_SYNC() do { asm volatile("s_waitcnt lgkmcnt(0)" ::: "memory"); BARRIER(); } while (0)

__device__ __forceinline__ bf8 lds_b128(unsigned a) { return *(LDS const bf8*)(size_t)a; }
__device__ __forceinline__ bf8 lds_tr(unsigned a) {
  bf4 lo = __builtin_amdgcn_ds_read_tr16_b64_v4bf16((LDS bf4*)(size_t)a);
  bf4 hi = __builtin_amdgcn_ds_read_tr16_b64_v4bf16((LDS bf4*)(size_t)(a + 512));
  return __builtin_shufflevector(lo, hi, 0, 1, 2, 3, 4, 5, 6, 7);
}
__device__ __forceinline__ f4 mfma(bf8 a, bf8 b, f4 c) { return __builtin_amdgcn_mfma_f32_16x16x32_bf16(a, b, c, 0, 0, 0); }
__device__ __forceinline__ float dot_ones(bf8 v, float c) {
  const bf2 one = {(__bf16)1.0f, (__bf16)1.0f};
#pragma unroll
  for (int i = 0; i < 4; ++i) c = __builtin_amdgcn_fdot2_f32_bf16((bf2){v[2 * i], v[2 * i + 1]}, one, c, false);
  return c;
}
__device__ __forceinline__ float dot_self(bf8 v, float c) {
#pragma unroll
  for (int i = 0; i < 4; ++i) { bf2 p = {v[2 * i], v[2 * i + 1]}; c = __builtin_amdgcn_fdot2_f32_bf16(p, p, c, false); }
  return c;
}
__device__ __forceinline__ float bfr(float v) { return (float)(__bf16)v; }
__device__ __forceinline__ float bf2f(u16 v) { return __uint_as_float((unsigned)v << 16); }
__device__ __forceinline__ unsigned pack2(float a, float b) { bf2 p = {(__bf16)a, (__bf16)b}; return __builtin_bit_cast(unsigned, p); }
__device__ __forceinline__ float sum_g(float v) { v += __shfl_xor(v, 16); v += __shfl_xor(v, 32); return v; }
__device__ __forceinline__ void lds_st(unsigned a, float v) { *(LDS float*)(size_t)a = v; }
__device__ __forceinline__ float lds_ld(unsigned a) { return *(LDS const float*)(size_t)a; }
__device__ __forceinline__ f4 lds_ld4(unsigned a) { return *(LDS const f4*)(size_t)a; }
__device__ __forceinline__ f2 lds_ld2(unsigned a) { return *(LDS const f2*)(size_t)a; }
__device__ __forceinline__ void lds_st2(unsigned a, f2 v) { *(LDS f2*)(size_t)a = v; }
__device__ __forceinline__ void lds_st4(unsigned a, f4 v) { *(LDS f4*)(size_t)a = v; }
__device__ __forceinline__ f2 cmul_add(f2 a, f2 b, f2 c) {
  return (f2){fmaf(-a.y, b.y, fmaf(a.x, b.x, c.x)), fmaf(a.y, b.x, fmaf(a.x, b.y, c.y))};
}
__device__ __forceinline__ f2 cmul(f2 a, f2 b) { return (f2){a.x * b.x - a.y * b.y, a.x * b.y + a.y * b.x}; }

struct Params {
  const u16* x; const u16* lre; const u16* lim; const u16* bre; const u16* bim; const u16* cre; const u16* cim;
  const u16* ldt; const u16* dsk; const u16* w1; const u16* w2; float* out; char* ws;
};

constexpr unsigned E_PART = 0;
constexpr unsigned E_MEAN = 4096;
constexpr unsigned E_RINV = 4608;
constexpr unsigned E_CS = 5120;
constexpr unsigned E_AGG = 8192;
constexpr unsigned E_CIN = 12288;
constexpr unsigned K1_CONST = 131072;
constexpr unsigned K1_LDS = K1_CONST + 1024;

template <bool SSM>
__device__ __forceinline__ void k1_body(const Params& p, int rt, int j, int kst) {
  constexpr int NT = SSM ? 2 : 3;
  constexpr int NSLOT = SSM ? 4 : 3;
  constexpr unsigned SLOT = SSM ? 32768 : 40960;
  const int tid = threadIdx.x, lane = tid & 63;
  const int w = __builtin_amdgcn_readfirstlane(tid >> 6);
  const int c = lane & 15, g = lane >> 4;
  const int t0 = rt * 128;
  STAMP_DECL; STAMP(0);
  int f_lo = 0, f_hi = 0, f_ld = 0;
  if (!SSM) { f_lo = 16 * ((86 * j) >> 3); f_hi = 16 * ((86 * (j + 1)) >> 3); f_ld = f_lo < F - 192 ? f_lo : F - 192; }

  if (w >= 8) {
    const int L = w - 8;
    unsigned r_lre = 0, r_lim = 0, r_ldt = 0, tag = 0;
    if (SSM && L == 0) {
      const unsigned o = (unsigned)(j * 64 + lane) * 2, z = 0;
      asm volatile("global_load_ushort %0, %4, %6\n\tglobal_load_ushort %1, %4, %7\n\tglobal_load_ushort %2, %4, %8\n\t"
                   "global_load_dword %3, %5, %9 sc1"
                   : "=&v"(r_lre), "=&v"(r_lim), "=&v"(r_ldt), "=&v"(tag)
                   : "v"(o), "v"(z), "s"(p.lre), "s"(p.lim), "s"(p.ldt), "s"(p.ws + WS_EPOCH + j * 128));
    }
    unsigned voA[4], voB[6];
#pragma unroll
    for (int u = 0; u < 4; ++u) {
      const int row = 32 * L + 8 * u + (lane >> 3), key = (row & 3) | ((row >> 2) & 4);
      voA[u] = (unsigned)(row * D + (((lane & 7) ^ key) << 3)) * 2 + 4096 - 1024 * u;
    }
    const char* baseA = (const char*)(p.x + (size_t)t0 * D);
    const char* baseB;
    if (SSM) {
#pragma unroll
      for (int u = 0; u < 4; ++u) {
        const int row = (32 * L + 8 * u + (lane >> 3)) & 63;
        voB[u] = (unsigned)(row * D + (((lane & 7) ^ (row & 7)) << 3)) * 2 + 4096 - 1024 * u;
      }
      baseB = (const char*)((L < 2 ? p.bre : p.bim) + (size_t)j * 64 * D);
    } else {
#pragma unroll
      for (int u = 0; u < 6; ++u) {
        const int i = 6 * L + u, k = 8 * (i & 7) + (lane >> 3), key = ((k >> 1) & 1) | ((k >> 2) & 2);
        voB[u] = (unsigned)(k * F + 64 * (i >> 3) + (((lane & 7) ^ (key << 1)) << 3)) * 2 + 4096 - 1024 * (u & 3);
      }
      baseB = (const char*)(p.w1 + f_ld);
    }
    auto stage = [&](int s) {
      const int ks = (s + kst) & 7;
      const unsigned slot = (unsigned)(s % NSLOT) * SLOT;
      glds_x4(voA, baseA + ks * 128, slot + L * 4096);
      if (SSM) {
        glds_x4(voB, baseB + ks * 128, slot + 16384 + L * 4096);
      } else {
        const char* b = baseB + (size_t)ks * 64 * F * 2;
        glds_x4(voB, b, slot + 16384 + L * 6144);
        glds_x2(voB + 4, b, slot + 16384 + L * 6144 + 4096);
      }
    };
#pragma unroll
    for (int s = 0; s < NSLOT - 1; ++s) stage(s);
#pragma unroll
    for (int s = 0; s < 8; ++s) {
      if (SSM) { if (s + 2 < 8) WAIT_VM(16); else if (s + 1 < 8) WAIT_VM(8); else WAIT_VM(0); }
      else     { if (s + 1 < 8) WAIT_VM(10); else WAIT_VM(0); }
      BARRIER();
      if (s + NSLOT - 1 < 8) stage(s + NSLOT - 1);
      if (SSM && s == 0 && L == 0) {
        asm volatile("" : "+v"(r_lre), "+v"(r_lim), "+v"(r_ldt), "+v"(tag));
        tag += 1u;
        const float lr = bf2f((u16)r_lre), li = bf2f((u16)r_lim);
        const float dt = bfr(expf(bf2f((u16)r_ldt)));
        const float e = expf(dt * lr);
        float sn, cn; sincosf(dt * li, &sn, &cn);
        const f2 lam = {e * cn, e * sn};
        const float nr = lam.x - 1.0f, ni = lam.y, den = lr * lr + li * li;
        lds_st2(K1_CONST + lane * 8, lam);
        lds_st2(K1_CONST + 512 + lane * 8, (f2){(nr * lr + ni * li) / den, (ni * lr - nr * li) / den});
      }
    }
    LDS_SYNC();
    LDS_SYNC();
    LDS_SYNC();
    if (SSM) {
      f2 l16 = {0.f, 0.f}, l128 = {0.f, 0.f};
      if (L == 0) {
        l16 = lds_ld2(K1_CONST + lane * 8);
#pragma unroll
        for (int i = 0; i < 4; ++i) l16 = cmul(l16, l16);
        l128 = l16;
#pragma unroll
        for (int i = 0; i < 3; ++i) l128 = cmul(l128, l128);
      }
      LDS_SYNC();
      if (L == 0) {
        f2 ag[8];
#pragma unroll
        for (int i = 0; i < 8; ++i) ag[i] = lds_ld2(E_AGG + (i * 64 + lane) * 8);
        f2 tot = ag[0];
#pragma unroll
        for (int i = 1; i < 8; ++i) tot = cmul_add(l16, tot, ag[i]);
        GLB u64* rec = (GLB u64*)(p.ws + WS_REC) + (size_t)j * 128 + lane;
        if (rt < 15) {
          __hip_atomic_store(rec + rt * 1024, ((u64)tag << 32) | __float_as_uint(tot.x), __ATOMIC_RELAXED, __HIP_MEMORY_SCOPE_AGENT);
          __hip_atomic_store(rec + rt * 1024 + 64, ((u64)tag << 32) | __float_as_uint(tot.y), __ATOMIC_RELAXED, __HIP_MEMORY_SCOPE_AGENT);
        }
        f2 carry = {0.f, 0.f};
        if (rt > 0) {
          u64 vr[15], vi[15];
          for (int spin = 0; spin < 400000; ++spin) {
            bool ok = true;
#pragma unroll
            for (int i = 0; i < 15; ++i) {
              const int ii = i < rt ? i : 0;
              vr[i] = __hip_atomic_load(rec + ii * 1024, __ATOMIC_RELAXED, __HIP_MEMORY_SCOPE_AGENT);
              vi[i] = __hip_atomic_load(rec + ii * 1024 + 64, __ATOMIC_RELAXED, __HIP_MEMORY_SCOPE_AGENT);
            }
#pragma unroll
            for (int i = 0; i < 15; ++i) ok = ok && (unsigned)(vr[i] >> 32) == tag && (unsigned)(vi[i] >> 32) == tag;
            if (__all(ok)) break;
            __builtin_amdgcn_s_sleep(2);
          }
#pragma unroll
          for (int i = 0; i < 15; ++i)
            if (i < rt) carry = cmul_add(l128, carry, (f2){__uint_as_float((unsigned)vr[i]), __uint_as_float((unsigned)vi[i])});
          if (rt == 15 && lane == 0)
            __hip_atomic_store((GLB unsigned*)(p.ws + WS_EPOCH) + j * 32, tag, __ATOMIC_RELAXED, __HIP_MEMORY_SCOPE_AGENT);
        }
#pragma unroll
        for (int i = 0; i < 8; ++i) {
          lds_st2(E_CIN + (i * 64 + lane) * 8, carry);
          carry = cmul_add(l16, carry, ag[i]);
        }
      }
      LDS_SYNC();
    }
    return;
  }

  const int m = w >> 2, n = w & 3;
  unsigned adA[2], adB[3];
  {
    const int row = 64 * m + 16 * (c >> 2) + (c & 3);
    adA[0] = row * 128 + ((g ^ (c & 7)) << 4);
    adA[1] = adA[0] ^ 64;
    if (SSM) {
      adB[0] = 16384 + (16 * n + c) * 128 + ((g ^ (c & 7)) << 4);
      adB[1] = adB[0] ^ 64;
    } else {
      const int q = c >> 2, pp = c & 3, key = ((q >> 1) & 1) | ((g & 1) << 1);
#pragma unroll
      for (int u = 0; u < 3; ++u) {
        const int ct = 3 * n + u;
        adB[u] = 16384 + (ct >> 2) * 8192 + (8 * g + q) * 128 + (((((ct & 3) ^ key) << 1) | (pp >> 1)) << 4) + 8 * (pp & 1);
      }
    }
  }
  f4 acc[4][NT];
#pragma unroll
  for (int a = 0; a < 4; ++a)
#pragma unroll
    for (int u = 0; u < NT; ++u) acc[a][u] = (f4){0.f, 0.f, 0.f, 0.f};
  float rs[4] = {0.f, 0.f, 0.f, 0.f}, rq[4] = {0.f, 0.f, 0.f, 0.f}, cs[NT];
#pragma unroll
  for (int u = 0; u < NT; ++u) cs[u] = 0.f;

#pragma unroll
  for (int s = 0; s < 8; ++s) {
    LDS_SYNC();
    STAMP(1 + s);
    const unsigned slot = (unsigned)(s % NSLOT) * SLOT;
#pragma unroll
    for (int ks = 0; ks < 2; ++ks) {
      bf8 A[4], B[NT];
#pragma unroll
      for (int a = 0; a < 4; ++a) A[a] = lds_b128(slot + adA[ks] + 512 * a);
      if (SSM) {
        B[0] = lds_b128(slot + adB[ks]);
        B[1] = lds_b128(slot + adB[ks] + 8192);
      } else {
#pragma unroll
        for (int u = 0; u < 3; ++u) B[u] = lds_tr(slot + adB[u] + ks * 4096);
      }
#pragma unroll
      for (int a = 0; a < 4; ++a)
#pragma unroll
        for (int u = 0; u < NT; ++u) acc[a][u] = mfma(A[a], B[u], acc[a][u]);
      if (((2 * s + ks + 2 * m) & 3) == n) {
#pragma unroll
        for (int a = 0; a < 4; ++a) { rs[a] = dot_ones(A[a], rs[a]); rq[a] = dot_self(A[a], rq[a]); }
      }
      if (ks == m) {
#pragma unroll
        for (int u = 0; u < NT; ++u) cs[u] = dot_ones(B[u], cs[u]);
      }
    }
  }
  LDS_SYNC();
  STAMP(9);

#pragma unroll
  for (int a = 0; a < 4; ++a) { rs[a] = sum_g(rs[a]); rq[a] = sum_g(rq[a]); }
#pragma unroll
  for (int u = 0; u < NT; ++u) cs[u] = sum_g(cs[u]);
  if (g == 0) {
#pragma unroll
    for (int a = 0; a < 4; ++a) {
      const int row = 64 * m + 16 * (c >> 2) + 4 * a + (c & 3);
      lds_st2(E_PART + (n * 128 + row) * 8, (f2){rs[a], rq[a]});
    }
#pragma unroll
    for (int u = 0; u < NT; ++u) lds_st(E_CS + ((m * 12 + n * NT + u) * 16 + c) * 4, cs[u]);
  }
  LDS_SYNC();
  if (tid < 128) {
    f2 s = lds_ld2(E_PART + tid * 8);
#pragma unroll
    for (int i = 1; i < 4; ++i) s += lds_ld2(E_PART + (i * 128 + tid) * 8);
    const float mean = s.x * (1.0f / D);
    const float var = fmaxf(s.y * (1.0f / D) - mean * mean, 0.f);
    const float mb = bfr(mean), den = bfr(sqrtf(bfr(bfr(var) + 1e-5f)));
    lds_st(E_MEAN + tid * 4, mb);
    lds_st(E_RINV + tid * 4, 1.0f / den);
    if (SSM && j == 0) ((f2*)(p.ws + WS_STAT))[t0 + tid] = (f2){mb, den};
  }
#pragma unroll
  for (int u = 0; u < NT; ++u)
    cs[u] = lds_ld(E_CS + ((n * NT + u) * 16 + c) * 4) + lds_ld(E_CS + ((12 + n * NT + u) * 16 + c) * 4);
  LDS_SYNC();
  f4 mean4[4], rinv4[4];
#pragma unroll
  for (int a = 0; a < 4; ++a) {
    mean4[a] = lds_ld4(E_MEAN + (64 * m + 16 * g + 4 * a) * 4);
    rinv4[a] = lds_ld4(E_RINV + (64 * m + 16 * g + 4 * a) * 4);
  }
#pragma unroll
  for (int a = 0; a < 4; ++a)
#pragma unroll
    for (int u = 0; u < NT; ++u)
#pragma unroll
      for (int i = 0; i < 4; ++i) acc[a][u][i] = (acc[a][u][i] - mean4[a][i] * cs[u]) * rinv4[a][i];

  STAMP(10);
  char* ht = p.ws + WS_H + (size_t)(2 * rt + m) * KH * 128 + g * 32;
  if (!SSM) {
#pragma unroll
    for (int u = 0; u < 3; ++u) {
      const int f = f_ld + 16 * (3 * n + u);
      if (f >= f_lo && f < f_hi) {
        u4 o[2];
#pragma unroll
        for (int a = 0; a < 4; ++a) {
          o[a >> 1][2 * (a & 1)] = pack2(fmaxf(acc[a][u][0], 0.f), fmaxf(acc[a][u][1], 0.f));
          o[a >> 1][2 * (a & 1) + 1] = pack2(fmaxf(acc[a][u][2], 0.f), fmaxf(acc[a][u][3], 0.f));
        }
        u4* dst = (u4*)(ht + (size_t)(1024 + f + c) * 128);
        dst[0] = o[0]; dst[1] = o[1];
      }
    }
  } else {
    const int chl = 16 * n + c, seg = 4 * m + g;
    const f2 lam = lds_ld2(K1_CONST + chl * 8), cc = lds_ld2(K1_CONST + 512 + chl * 8);
    f2 st = {0.f, 0.f};
#pragma unroll
    for (int a = 0; a < 4; ++a)
#pragma unroll
      for (int i = 0; i < 4; ++i) {
        const f2 bu = cmul(cc, (f2){acc[a][0][i], acc[a][1][i]});
        acc[a][0][i] = bu.x; acc[a][1][i] = bu.y;
        st = cmul_add(lam, st, bu);
      }
    lds_st2(E_AGG + (seg * 64 + chl) * 8, st);
    LDS_SYNC();
    STAMP(11);
    LDS_SYNC();
    STAMP(12);
    f2 h = lds_ld2(E_CIN + (seg * 64 + chl) * 8);
    u4 ore[2], oim[2];
#pragma unroll
    for (int a = 0; a < 4; ++a) {
      float hr[4], hi[4];
#pragma unroll
      for (int i = 0; i < 4; ++i) {
        h = cmul_add(lam, h, (f2){acc[a][0][i], acc[a][1][i]});
        hr[i] = 2.0f * h.x; hi[i] = -2.0f * h.y;
      }
      ore[a >> 1][2 * (a & 1)] = pack2(hr[0], hr[1]); ore[a >> 1][2 * (a & 1) + 1] = pack2(hr[2], hr[3]);
      oim[a >> 1][2 * (a & 1)] = pack2(hi[0], hi[1]); oim[a >> 1][2 * (a & 1) + 1] = pack2(hi[2], hi[3]);
    }
    u4* dre = (u4*)(ht + (size_t)(j * 64 + chl) * 128);
    u4* dim = (u4*)(ht + (size_t)(512 + j * 64 + chl) * 128);
    dre[0] = ore[0]; dre[1] = ore[1]; dim[0] = oim[0]; dim[1] = oim[1];
  }
  STAMP(13);
  STAMP(14);
  STAMP_OUT(0);
}

__global__ __launch_bounds__(768, 3) void s5_k1(Params p) {
  const int b = blockIdx.x, xcd = b & 7, idx = b >> 3;
  const int rt = 2 * xcd + (idx >> 4), j = idx & 15;
  const int kst = (idx + (idx >> 4) * 4) & 7;
  if (j < 8) k1_body<true>(p, rt, j, kst);
  else k1_body<false>(p, rt, j - 8, kst);
}

constexpr unsigned K2_LDS = 131072;
__global__ __launch_bounds__(512, 2) void s5_k2(Params p) {
  const int b = blockIdx.x, xcd = b & 7, idx = b >> 3;
  const int tt = 4 * xcd + (idx >> 3), dt = idx & 7;
  const int tid = threadIdx.x, lane = tid & 63;
  const int w = __builtin_amdgcn_readfirstlane(tid >> 6);
  const int c = lane & 15, g = lane >> 4;
  const int d0 = dt * 64, t0 = tt * 64;
  const int s0 = (idx * 5) % 19;
  STAMP_DECL; STAMP(0);

  u2 xr[2], sr[2], dr;
  {
    const unsigned ox = (unsigned)((8 * w + g) * D + d0 + 4 * c) * 2, os = (unsigned)(8 * w + g) * 8, od = (unsigned)(d0 + 4 * c) * 2;
    const char* xb = (const char*)(p.x + (size_t)t0 * D);
    const char* sb = p.ws + WS_STAT + (size_t)t0 * 8;
    asm volatile("global_load_dwordx2 %0, %5, %8\n\tglobal_load_dwordx2 %1, %5, %9\n\t"
                 "global_load_dwordx2 %2, %6, %10\n\tglobal_load_dwordx2 %3, %6, %10 offset:32\n\tglobal_load_dwordx2 %4, %7, %11"
                 : "=&v"(xr[0]), "=&v"(xr[1]), "=&v"(sr[0]), "=&v"(sr[1]), "=&v"(dr)
                 : "v"(ox), "v"(os), "v"(od), "s"(xb), "s"(xb + 4 * D * 2), "s"(sb), "s"(p.dsk));
  }
  f4 acc[4][4];
  if (w >= 4) {
    const int L = w - 4;
    unsigned voH[4], voN[4], voK[4];
#pragma unroll
    for (int u = 0; u < 4; ++u) {
      const int i = 4 * L + u, r8 = lane >> 3, pos = lane & 7;
      const int k = 8 * i + r8, key = ((k >> 1) & 1) | ((k >> 2) & 2);
      const int chunk = pos ^ (key << 1);
      voH[u] = (unsigned)(k * 128 + chunk * 16) + 4096 - 1024 * u;
      voK[u] = (unsigned)(k * (D * 2) + chunk * 16) + 4096 - 1024 * u;
      const int row = 8 * (i & 7) + r8;
      voN[u] = (unsigned)(row * (P * 2) + (i >> 3) * 128 + ((pos ^ (row & 7)) << 4)) + 4096 - 1024 * u;
    }
    const char* hbase = p.ws + WS_H + (size_t)tt * KH * 128;
    auto stage = [&](int s) {
      int st = s + s0; st = st >= 19 ? st - 19 : st;
      const unsigned slot = (unsigned)(s & 3) * 32768;
      if (st < 8) {
        glds_x4(voN, (const char*)((st < 4 ? p.cre : p.cim) + (size_t)d0 * P + (st & 3) * 128), slot + L * 4096);
      } else {
        const char* wb = (const char*)(p.w2 + (size_t)(st - 8) * 128 * D + d0);
        if (st == 18 && L == 3) wb -= 32 * D * 2;
        glds_x4(voK, wb, slot + L * 4096);
      }
      glds_x4(voH, hbase + (size_t)st * 16384, slot + 16384 + L * 4096);
    };
    stage(0); stage(1); stage(2);
    for (int s = 0; s < 19; ++s) {
      if (s < 17) WAIT_VM(16); else if (s == 17) WAIT_VM(8); else WAIT_VM(0);
      BARRIER();
      if (s + 3 < 19) stage(s + 3);
    }
    LDS_SYNC();
  } else {
    unsigned adT[4], adN;
    const int q = c >> 2, pp = c & 3, key = ((q >> 1) & 1) | ((g & 1) << 1);
#pragma unroll
    for (int u = 0; u < 4; ++u) adT[u] = (32 * w + 8 * g + q) * 128 + ((((u ^ key) << 1) | (pp >> 1)) << 4) + 8 * (pp & 1);
    adN = (w >> 1) * 8192 + c * 128 + (((4 * (w & 1) + g) ^ (c & 7)) << 4);
#pragma unroll
    for (int a = 0; a < 4; ++a)
#pragma unroll
      for (int u = 0; u < 4; ++u) acc[a][u] = (f4){0.f, 0.f, 0.f, 0.f};
    for (int s = 0; s < 19; ++s) {
      LDS_SYNC();
      int st = s + s0; st = st >= 19 ? st - 19 : st;
      const unsigned slot = (unsigned)(s & 3) * 32768;
      if (st == 18 && w == 3) continue;
      bf8 W[4], H[4];
      if (st < 8) {
#pragma unroll
        for (int a = 0; a < 4; ++a) W[a] = lds_b128(slot + adN + a * 2048);
      } else {
#pragma unroll
        for (int a = 0; a < 4; ++a) W[a] = lds_tr(slot + adT[a]);
      }
#pragma unroll
      for (int u = 0; u < 4; ++u) H[u] = lds_tr(slot + 16384 + adT[u]);
#pragma unroll
      for (int a = 0; a < 4; ++a)
#pragma unroll
        for (int u = 0; u < 4; ++u) acc[a][u] = mfma(W[a], H[u], acc[a][u]);
    }
    LDS_SYNC();
    STAMP(8);
#pragma unroll
    for (int a = 0; a < 4; ++a)
#pragma unroll
      for (int u = 0; u < 4; ++u) {
        const int t = 16 * u + c;
        lds_st4(w * 16384 + t * 256 + (((4 * a + g) ^ (t & 7)) << 4), acc[a][u]);
      }
  }
  LDS_SYNC();
  asm volatile("s_waitcnt vmcnt(0)" : "+v"(xr[0]), "+v"(xr[1]), "+v"(sr[0]), "+v"(sr[1]), "+v"(dr) :: "memory");
  const f4 dsk = {__uint_as_float(dr.x << 16), __uint_as_float(dr.x & 0xffff0000u), __uint_as_float(dr.y << 16), __uint_as_float(dr.y & 0xffff0000u)};
#pragma unroll
  for (int r = 0; r < 2; ++r) {
    const int t = 8 * w + 4 * r + g;
    const unsigned a = t * 256 + ((c ^ (t & 7)) << 4);
    f4 v = lds_ld4(a) + lds_ld4(a + 16384) + lds_ld4(a + 32768) + lds_ld4(a + 49152);
    const f2 stt = {__uint_as_float(sr[r].x), __uint_as_float(sr[r].y)};
    const float xv[4] = {__uint_as_float(xr[r].x << 16), __uint_as_float(xr[r].x & 0xffff0000u),
                         __uint_as_float(xr[r].y << 16), __uint_as_float(xr[r].y & 0xffff0000u)};
#pragma unroll
    for (int i = 0; i < 4; ++i) {
      const float xn = bfr(bfr(xv[i] - stt.x) / stt.y);
      v[i] = xv[i] + (v[i] + bfr(xn * dsk[i]));
    }
    *(f4*)(p.out + (size_t)(t0 + t) * D + d0 + 4 * c) = v;
  }
  STAMP(9);
  STAMP(10);
  STAMP_OUT(1);
}

extern "C" void kernel_launch(void* const* d_in, const int* in_sizes, int n_in, void* d_out, int out_size,
                              void* d_ws, size_t ws_size, hipStream_t stream) {
  Params p;
  p.x = (const u16*)d_in[0]; p.lre = (const u16*)d_in[1]; p.lim = (const u16*)d_in[2];
  p.bre = (const u16*)d_in[3]; p.bim = (const u16*)d_in[4]; p.cre = (const u16*)d_in[5]; p.cim = (const u16*)d_in[6];
  p.ldt = (const u16*)d_in[7]; p.dsk = (const u16*)d_in[8]; p.w1 = (const u16*)d_in[10]; p.w2 = (const u16*)d_in[11];
  p.out = (float*)d_out; p.ws = (char*)d_ws;
  (void)hipFuncSetAttribute((const void*)s5_k1, hipFuncAttributeMaxDynamicSharedMemorySize, K1_LDS);
  (void)hipFuncSetAttribute((const void*)s5_k2, hipFuncAttributeMaxDynamicSharedMemorySize, K2_LDS);
  s5_k1<<<256, 768, K1_LDS, stream>>>(p);
  s5_k2<<<256, 512, K2_LDS, stream>>>(p);
}
```

```cpp
#include <hip/hip_runtime.h>
#include <hip/hip_bf16.h>
#include <stdint.h>

typedef __attribute__((ext_vector_type(8))) __bf16 bf8;
typedef __attribute__((ext_vector_type(4))) __bf16 bf4;
typedef __attribute__((ext_vector_type(2))) __bf16 bf2;
typedef __attribute__((ext_vector_type(4))) float f4;
typedef __attribute__((ext_vector_type(2))) float f2;
typedef __attribute__((ext_vector_type(4))) unsigned u4;
typedef __attribute__((ext_vector_type(2))) unsigned u2;
typedef __attribute__((ext_vector_type(2))) short s2;
typedef unsigned long long u64;
typedef unsigned short u16;
#define LDS __attribute__((address_space(3)))
#define GLB __attribute__((address_space(1)))

constexpr int T = 2048, D = 512, P = 512, F = 1376;
constexpr int KH = 2432;
constexpr size_t WS_EPOCH = 0;
constexpr size_t WS_FLAG = 4096;
constexpr size_t WS_ROW = 8192;
constexpr size_t WS_REC = 65536;
constexpr size_t WS_STAMP = 128u << 20;
#define STAMP_DECL
#define STAMP(i)
#define STAMP_OUT(k)
constexpr size_t WS_H = 1 << 20;

__device__ __forceinline__ void glds_x4(const unsigned* vo, const char* sbase, unsigned lds_addr) {
  unsigned keep;
  asm volatile("s_mov_b32 %0, m0\n\ts_mov_b32 m0, %6\n\ts_nop 0\n\tglobal_load_lds_dwordx4 %1, %5\n\t"
               "global_load_lds_dwordx4 %2, %5 offset:1024\n\tglobal_load_lds_dwordx4 %3, %5 offset:2048\n\t"
               "global_load_lds_dwordx4 %4, %5 offset:3072\n\ts_mov_b32 m0, %0"
               : "=&s"(keep) : "v"(vo[0]), "v"(vo[1]), "v"(vo[2]), "v"(vo[3]), "s"(sbase - 4096), "s"(lds_addr));
}
__device__ __forceinline__ void glds_x4_dev(const unsigned* vo, const char* sbase, unsigned lds_addr) {
  unsigned keep;
  asm volatile("s_mov_b32 %0, m0\n\ts_mov_b32 m0, %6\n\ts_nop 0\n\tglobal_load_lds_dwordx4 %1, %5 sc1\n\t"
               "global_load_lds_dwordx4 %2, %5 offset:1024 sc1\n\tglobal_load_lds_dwordx4 %3, %5 offset:2048 sc1\n\t"
               "global_load_lds_dwordx4 %4, %5 offset:3072 sc1\n\ts_mov_b32 m0, %0"
               : "=&s"(keep) : "v"(vo[0]), "v"(vo[1]), "v"(vo[2]), "v"(vo[3]), "s"(sbase - 4096), "s"(lds_addr));
}
__device__ __forceinline__ void glds_x2(const unsigned* vo, const char* sbase, unsigned lds_addr) {
  unsigned keep;
  asm volatile("s_mov_b32 %0, m0\n\ts_mov_b32 m0, %4\n\ts_nop 0\n\tglobal_load_lds_dwordx4 %1, %3\n\t"
               "global_load_lds_dwordx4 %2, %3 offset:1024\n\ts_mov_b32 m0, %0"
               : "=&s"(keep) : "v"(vo[0]), "v"(vo[1]), "s"(sbase - 4096), "s"(lds_addr));
}
__device__ __forceinline__ void touch(unsigned voff, const void* sbase, unsigned lds_addr) {
  unsigned keep;
  asm volatile("s_mov_b32 %0, m0\n\ts_mov_b32 m0, %3\n\ts_nop 0\n\tglobal_load_lds_dword %1, %2\n\ts_mov_b32 m0, %0"
               : "=&s"(keep) : "v"(voff), "s"(sbase), "s"(lds_addr));
}
constexpr int NPF = 3;
template <int N> __device__ __forceinline__ void wait_vm() { asm volatile("s_waitcnt vmcnt(%0) lgkmcnt(0)" :: "n"(N) : "memory"); }
#define WAIT_VM(n) asm volatile("s_waitcnt vmcnt(" #n ") lgkmcnt(0)" ::: "memory")
#define BARRIER() do { __builtin_amdgcn_s_barrier(); asm volatile("" ::: "memory"); } while (0)
#define SCHED_FENCE() __builtin_amdgcn_sched_barrier(0)
#define LDS_SYNC() do { asm volatile("s_waitcnt lgkmcnt(0)" ::: "memory"); BARRIER(); } while (0)

__device__ __forceinline__ bf8 lds_b128(unsigned a) { return *(LDS const bf8*)(size_t)a; }
__device__ __forceinline__ bf8 lds_tr(unsigned a) {
  bf4 lo = __builtin_amdgcn_ds_read_tr16_b64_v4bf16((LDS bf4*)(size_t)a);
  bf4 hi = __builtin_amdgcn_ds_read_tr16_b64_v4bf16((LDS bf4*)(size_t)(a + 512));
  return __builtin_shufflevector(lo, hi, 0, 1, 2, 3, 4, 5, 6, 7);
}
__device__ __forceinline__ f4 mfma(bf8 a, bf8 b, f4 c) { return __builtin_amdgcn_mfma_f32_16x16x32_bf16(a, b, c, 0, 0, 0); }
__device__ __forceinline__ float bfr(float v) { return (float)(__bf16)v; }
__device__ __forceinline__ float bf2f(u16 v) { return __uint_as_float((unsigned)v << 16); }
__device__ __forceinline__ unsigned pack2(float a, float b) { return __builtin_bit_cast(unsigned, __builtin_convertvector((f2){a, b}, bf2)); }
__device__ __forceinline__ void lds_st(unsigned a, float v) { *(LDS float*)(size_t)a = v; }
__device__ __forceinline__ float lds_ld(unsigned a) { return *(LDS const float*)(size_t)a; }
__device__ __forceinline__ f4 lds_ld4(unsigned a) { return *(LDS const f4*)(size_t)a; }
__device__ __forceinline__ f2 lds_ld2(unsigned a) { return *(LDS const f2*)(size_t)a; }
__device__ __forceinline__ void lds_st2(unsigned a, f2 v) { *(LDS f2*)(size_t)a = v; }
__device__ __forceinline__ void lds_st4(unsigned a, f4 v) { *(LDS f4*)(size_t)a = v; }
__device__ __forceinline__ f2 cmul_add(f2 a, f2 b, f2 c) {
  return (f2){fmaf(-a.y, b.y, fmaf(a.x, b.x, c.x)), fmaf(a.y, b.x, fmaf(a.x, b.y, c.y))};
}
__device__ __forceinline__ f2 cmul(f2 a, f2 b) { return (f2){a.x * b.x - a.y * b.y, a.x * b.y + a.y * b.x}; }

struct Params {
  const u16* x; const u16* lre; const u16* lim; const u16* bre; const u16* bim; const u16* cre; const u16* cim;
  const u16* ldt; const u16* dsk; const u16* w1; const u16* w2; float* out; char* ws;
};

constexpr unsigned C_LAM = 131072;
constexpr unsigned C_MEAN = C_LAM + 1024;
constexpr unsigned C_RINV = C_MEAN + 512;
constexpr unsigned C_CS = C_RINV + 512;
constexpr unsigned C_TAG = C_CS + 1536;
constexpr unsigned C_AGG = C_TAG + 256;
constexpr unsigned C_CIN = C_AGG + 4096;
constexpr unsigned C_JUNK = C_CIN + 4096;
constexpr unsigned LDS_BYTES = C_JUNK + 256;
constexpr int SPIN_MAX = 400000;
constexpr int DRAIN_S = 2;

template <int CTRL> __device__ __forceinline__ float dpp_add(float v) {
  return v + __int_as_float(__builtin_amdgcn_update_dpp(0, __float_as_int(v), CTRL, 0xf, 0xf, true));
}
__device__ __forceinline__ float wave_sum(float v) {
  v = dpp_add<0x111>(v); v = dpp_add<0x112>(v); v = dpp_add<0x114>(v); v = dpp_add<0x118>(v);
  v += __int_as_float(__builtin_amdgcn_update_dpp(0, __float_as_int(v), 0x142, 0xa, 0xf, true));
  v += __int_as_float(__builtin_amdgcn_update_dpp(0, __float_as_int(v), 0x143, 0xc, 0xf, true));
  return __int_as_float(__builtin_amdgcn_readlane(__float_as_int(v), 63));
}
__device__ __forceinline__ void st_gran(char* base, unsigned idx, unsigned tag, unsigned val) {
  __hip_atomic_store((GLB u64*)base + idx, ((u64)tag << 32) | val, __ATOMIC_RELAXED, __HIP_MEMORY_SCOPE_AGENT);
}
__device__ __forceinline__ u64 ld_gran(char* base, unsigned idx) {
  return __hip_atomic_load((GLB u64*)base + idx, __ATOMIC_RELAXED, __HIP_MEMORY_SCOPE_AGENT);
}
__device__ __forceinline__ unsigned wait_gran(char* base, unsigned idx, unsigned tag, u64 v) {
  if (!__all((unsigned)(v >> 32) == tag)) {
    int spin = 0;
    do {
      __builtin_amdgcn_s_sleep(1);
      v = ld_gran(base, idx);
    } while (!__all((unsigned)(v >> 32) == tag) && ++spin < SPIN_MAX);
  }
  return (unsigned)v;
}
__device__ __forceinline__ void st16_dev(char* base, unsigned off, u4 v) {
  asm volatile("global_store_dwordx4 %0, %1, %2 sc1\n\ts_nop 1" :: "v"(off), "v"(v), "s"(base) : "memory");
}
__device__ __forceinline__ unsigned ror8(unsigned v) { return (unsigned)__builtin_amdgcn_update_dpp(0, (int)v, 0x128, 0xf, 0xf, true); }
__device__ __forceinline__ void store_tile(char* ht, unsigned k0, int c, int g, u4 lo, u4 hi) {
  const bool low = c < 8;
  u4 a, b;
#pragma unroll
  for (int i = 0; i < 4; ++i) {
    const unsigned wv = ror8(low ? hi[i] : lo[i]);
    a[i] = low ? lo[i] : wv; b[i] = low ? wv : hi[i];
  }
  const unsigned off = (k0 + (c & 7)) * 128 + (2 * g + (c >> 3)) * 16;
  st16_dev(ht, off, a);
  st16_dev(ht, off + 1024, b);
}
__device__ __forceinline__ void unpack8(u4 v, float* f) {
#pragma unroll
  for (int i = 0; i < 4; ++i) { f[2 * i] = __uint_as_float(v[i] << 16); f[2 * i + 1] = __uint_as_float(v[i] & 0xffff0000u); }
}

template <bool SSM>
__device__ __forceinline__ void body(const Params& p, int rt, int j, int i8) {
  constexpr int NT = SSM ? 2 : 3;
  constexpr int NSLOT = SSM ? 4 : 3;
  constexpr unsigned SLOT = SSM ? 32768 : 40960;
  const int tid = threadIdx.x, lane = tid & 63;
  const int w = __builtin_amdgcn_readfirstlane(tid >> 6);
  const int c = lane & 15, g = lane >> 4;
  const int t0 = rt * 128;
  const int jj = SSM ? j : j + 8;
  const int hf = j >> 2, r4 = rt & 3;
  const int tt = 2 * rt + hf, d0 = i8 * 64;
  STAMP_DECL; STAMP(0);
  int f_lo = 0, f_hi = 0, f_ld = 0;
  if (!SSM) { f_lo = 16 * ((86 * j) >> 3); f_hi = 16 * ((86 * (j + 1)) >> 3); f_ld = f_lo < F - 192 ? f_lo : F - 192; }

  if (w >= 8) {
    const int L = w - 8;
    unsigned r_lre = 0, r_lim = 0, r_ldt = 0;
    if (SSM && L == 0) {
      const unsigned o = (unsigned)(j * 64 + lane) * 2;
      asm volatile("global_load_ushort %0, %3, %4\n\tglobal_load_ushort %1, %3, %5\n\tglobal_load_ushort %2, %3, %6"
                   : "=&v"(r_lre), "=&v"(r_lim), "=&v"(r_ldt) : "v"(o), "s"(p.lre), "s"(p.lim), "s"(p.ldt));
    }
    unsigned voA[4], voB[6];
#pragma unroll
    for (int u = 0; u < 4; ++u) {
      const int row = 32 * L + 8 * u + (lane >> 3), key = (row & 3) | ((row >> 2) & 4);
      voA[u] = (unsigned)(row * D + (((lane & 7) ^ key) << 3)) * 2 + 4096 - 1024 * u;
    }
    const char* baseA = (const char*)(p.x + (size_t)t0 * D);
    const char* baseB;
    if (SSM) {
#pragma unroll
      for (int u = 0; u < 4; ++u) {
        const int row = (32 * L + 8 * u + (lane >> 3)) & 63;
        voB[u] = (unsigned)(row * D + (((lane & 7) ^ (row & 7)) << 3)) * 2 + 4096 - 1024 * u;
      }
      baseB = (const char*)((L < 2 ? p.bre : p.bim) + (size_t)j * 64 * D);
    } else {
#pragma unroll
      for (int u = 0; u < 6; ++u) {
        const int i = 6 * L + u, k = 8 * (i & 7) + (lane >> 3), key = ((k >> 1) & 1) | ((k >> 2) & 2);
        voB[u] = (unsigned)(k * F + 64 * (i >> 3) + (((lane & 7) ^ (key << 1)) << 3)) * 2 + 4096 - 1024 * (u & 3);
      }
      baseB = (const char*)(p.w1 + f_ld);
    }
    auto stage = [&](int s) {
      const int ks = s;
      const unsigned slot = (unsigned)(s % NSLOT) * SLOT;
      glds_x4(voA, baseA + ks * 128, slot + L * 4096);
      if (SSM) {
        glds_x4(voB, baseB + ks * 128, slot + 16384 + L * 4096);
      } else {
        const char* b = baseB + (size_t)ks * 64 * F * 2;
        glds_x4(voB, b, slot + 16384 + L * 6144);
        glds_x2(voB + 4, b, slot + 16384 + L * 6144 + 4096);
      }
    };
#pragma unroll
    for (int s = 0; s < NSLOT - 1; ++s) stage(s);
    char* const gbase = p.ws + WS_ROW;
    const unsigned gi0 = t0 + 64 * (L & 1) + lane;
    u64 g0 = 0;
    unsigned tag = 0;
#pragma unroll
    for (int s = 0; s < 8; ++s) {
      if (SSM) { if (s < 5) WAIT_VM(16); else if (s == 5) wait_vm<17 + NPF>(); else if (s == 6) wait_vm<9 + NPF>(); else wait_vm<NPF>(); }
      else     { if (s < 6) WAIT_VM(10); else if (s == 6) wait_vm<10 + NPF>(); else wait_vm<NPF>(); }
      BARRIER();
      if (s == 4) asm volatile("global_load_dwordx2 %0, %1, %2 sc1" : "=&v"(g0) : "v"(gi0 * 8), "s"(gbase));
      if (s + NSLOT - 1 < 8) stage(s + NSLOT - 1);
      if (s + NSLOT - 1 == 7) {
        const unsigned sh = r4 * 8 + i8;
        touch((sh * 128 + (L & 1) * 64 + lane) * 128, L < 2 ? p.cre : p.cim, C_JUNK);
        touch((sh * 344 + L * 86 + lane) * 128, p.w2, C_JUNK);
        touch((sh * 344 + L * 86 + 64 + (lane < 22 ? lane : 21)) * 128, p.w2, C_JUNK);
      }
      if (s == 0) {
        tag = *(LDS const unsigned*)(size_t)C_TAG;
        if (SSM && L == 0) {
          asm volatile("" : "+v"(r_lre), "+v"(r_lim), "+v"(r_ldt));
          const float lr = bf2f((u16)r_lre), li = bf2f((u16)r_lim);
          const float dt = bfr(expf(bf2f((u16)r_ldt)));
          const float e = expf(dt * lr);
          float sn, cn; sincosf(dt * li, &sn, &cn);
          const f2 lam = {e * cn, e * sn};
          const float nr = lam.x - 1.0f, ni = lam.y, den = lr * lr + li * li;
          const float inv = 2.0f / den;
          float n0 = fmaf(nr, lr, ni * li), n1 = fmaf(ni, lr, -nr * li);
          asm volatile("" : "+v"(n0), "+v"(n1));
          lds_st2(C_LAM + lane * 8, (f2){lam.x, -lam.y});
          lds_st2(C_LAM + 512 + lane * 8, (f2){n0 * inv, n1 * inv});
        }
      }
      if (s == 7) {
        asm volatile("" : "+v"(g0));
        if (L < 2) {
          const unsigned v0 = wait_gran(gbase, gi0, tag, g0);
          lds_st(C_MEAN + (64 * L + lane) * 4, __uint_as_float(v0 & 0xffff0000u));
          lds_st(C_RINV + (64 * L + lane) * 4, 1.0f / __uint_as_float(v0 << 16));
        }
      }
    }
    LDS_SYNC();

    unsigned voH[4], voN[4], voK[4];
#pragma unroll
    for (int u = 0; u < 4; ++u) {
      const int i = 4 * L + u, r8 = lane >> 3, pos = lane & 7;
      const int k = 8 * i + r8, key = ((k >> 1) & 1) | ((k >> 2) & 2);
      const int chunk = pos ^ (key << 1);
      voH[u] = (unsigned)(k * 128 + chunk * 16) + 4096 - 1024 * u;
      voK[u] = (unsigned)(k * (D * 2) + chunk * 16) + 4096 - 1024 * u;
      const int row = 8 * (i & 7) + r8;
      voN[u] = (unsigned)(row * (P * 2) + (i >> 3) * 128 + ((pos ^ (row & 7)) << 4)) + 4096 - 1024 * u;
    }
    const char* hbase = p.ws + WS_H + (size_t)tt * KH * 128;
    auto stageW = [&](int s) {
      const unsigned slot = (unsigned)(s & 3) * 32768;
      if (s >= 11) {
        const int st = s - 11;
        glds_x4(voN, (const char*)((st < 4 ? p.cre : p.cim) + (size_t)d0 * P + (st & 3) * 128), slot + L * 4096);
      } else {
        const char* wb = (const char*)(p.w2 + (size_t)s * 128 * D + d0);
        if (s == 10 && L == 3) wb -= 32 * D * 2;
        glds_x4(voK, wb, slot + L * 4096);
      }
    };
    auto stageH = [&](int s) {
      const int kb = s >= 11 ? s - 11 : s + 8;
      glds_x4_dev(voH, hbase + (size_t)kb * 16384, (unsigned)(s & 3) * 32768 + 16384 + L * 4096);
    };
    if (!(SSM && L == 0)) { stageW(0); stageW(1); stageW(2); }
    if (SSM) {
      f2 l16 = {0.f, 0.f}, l128 = {0.f, 0.f};
      if (L == 0) {
        l16 = lds_ld2(C_LAM + lane * 8);
#pragma unroll
        for (int i = 0; i < 4; ++i) l16 = cmul(l16, l16);
        l128 = l16;
#pragma unroll
        for (int i = 0; i < 3; ++i) l128 = cmul(l128, l128);
      }
      LDS_SYNC();
      if (L == 0) {
        f2 ag[8];
#pragma unroll
        for (int i = 0; i < 8; ++i) ag[i] = lds_ld2(C_AGG + (i * 64 + lane) * 8);
        f2 tot = ag[0];
#pragma unroll
        for (int i = 1; i < 8; ++i) tot = cmul_add(l16, tot, ag[i]);
        char* rec = p.ws + WS_REC + (size_t)j * 1024;
        if (rt < 15) {
          st_gran(rec, rt * 1024 + lane, tag, __float_as_uint(tot.x));
          st_gran(rec, rt * 1024 + 64 + lane, tag, __float_as_uint(tot.y));
        }
        f2 carry = {0.f, 0.f};
        if (rt > 0) {
          u64 vr[15], vi[15];
          for (int spin = 0; spin < SPIN_MAX; ++spin) {
            bool ok = true;
#pragma unroll
            for (int i = 0; i < 15; ++i) {
              const int ii = i < rt ? i : 0;
              vr[i] = ld_gran(rec, ii * 1024 + lane);
              vi[i] = ld_gran(rec, ii * 1024 + 64 + lane);
            }
#pragma unroll
            for (int i = 0; i < 15; ++i) ok = ok && (unsigned)(vr[i] >> 32) == tag && (unsigned)(vi[i] >> 32) == tag;
            if (__all(ok)) break;
            __builtin_amdgcn_s_sleep(1);
          }
#pragma unroll
          for (int i = 0; i < 15; ++i)
            if (i < rt) carry = cmul_add(l128, carry, (f2){__uint_as_float((unsigned)vr[i]), __uint_as_float((unsigned)vi[i])});
        }
#pragma unroll
        for (int i = 0; i < 8; ++i) {
          lds_st2(C_CIN + (i * 64 + lane) * 8, carry);
          carry = cmul_add(l16, carry, ag[i]);
        }
      }
      LDS_SYNC();
      if (L == 0) { stageW(0); stageW(1); stageW(2); }
    }
    char* const flags = p.ws + WS_FLAG;
    if (!SSM) {
      BARRIER();
      if (L == 0 && lane == 0) st_gran(flags, rt * 16 + jj, tag, 0);
    }
    {
      const unsigned fi = rt * 16 + 8 + (lane & 7);
      u64 v;
      asm volatile("global_load_dwordx2 %0, %1, %2 sc1\n\ts_waitcnt vmcnt(0)" : "=&v"(v) : "v"(fi * 8), "s"(flags) : "memory");
      wait_gran(flags, fi, tag, v);
      asm volatile("s_waitcnt vmcnt(0)" ::: "memory");
    }
    stageH(0); stageH(1); stageH(2);
    u64 fl;
#pragma unroll
    for (int s = 0; s < 19; ++s) {
      if (s == 0) WAIT_VM(8); else if (s == 1) WAIT_VM(12);
      else if (s == 6 || s == 7) WAIT_VM(17);
      else if (s < 17) WAIT_VM(16); else if (s == 17) WAIT_VM(8); else WAIT_VM(0);
      BARRIER();
      if (s == 8) {
        asm volatile("" : "+v"(fl));
        wait_gran(flags, rt * 16 + (lane & 7), tag, fl);
      }
      if (s + 3 < 19) { stageW(s + 3); stageH(s + 3); }
      if (SSM && s == DRAIN_S && L == 0 && lane == 0) st_gran(flags, rt * 16 + jj, tag, 0);
      if (s == 5) asm volatile("global_load_dwordx2 %0, %1, %2 sc1" : "=&v"(fl) : "v"((rt * 16 + (lane & 7)) * 8), "s"(flags));
    }
    if (L == 1 && rt == 0 && jj == 0) {
#pragma unroll
      for (int i = 0; i < 4; ++i) wait_gran(p.ws + WS_ROW, (i * 64 + lane) * 8, tag, ld_gran(p.ws + WS_ROW, (i * 64 + lane) * 8));
      if (lane == 0) __hip_atomic_store((GLB unsigned*)(p.ws + WS_EPOCH), tag, __ATOMIC_RELAXED, __HIP_MEMORY_SCOPE_AGENT);
    }
    LDS_SYNC();
  } else {
    const int m = w >> 2, n = w & 3;
    unsigned rowv;
    {
      u4 xv;
      unsigned ep;
      const unsigned xo = (unsigned)((8 * jj + w) * D + lane * 8) * 2, z = 0;
      asm volatile("global_load_dwordx4 %0, %2, %4\n\tglobal_load_dword %1, %3, %5 sc1\n\ts_waitcnt vmcnt(0)"
                   : "=&v"(xv), "=&v"(ep) : "v"(xo), "v"(z), "s"(p.x + (size_t)t0 * D), "s"(p.ws + WS_EPOCH) : "memory");
      if (w == 0 && lane == 0) *(LDS unsigned*)(size_t)C_TAG = ep + 1u;
      float f[8];
      unpack8(xv, f);
      float sm = 0.f;
#pragma unroll
      for (int i = 0; i < 8; ++i) sm += f[i];
      const float mean = wave_sum(sm) * (1.0f / D);
      float sq = 0.f;
#pragma unroll
      for (int i = 0; i < 8; ++i) { const float dd = f[i] - mean; sq = fmaf(dd, dd, sq); }
      const float var = wave_sum(sq) * (1.0f / D);
      const float mb = bfr(mean), den = bfr(sqrtf(bfr(bfr(var) + 1e-5f)));
      rowv = (__float_as_uint(mb) & 0xffff0000u) | (__float_as_uint(den) >> 16);
      unsigned adA[2], adB[3];
      {
        const int row = 64 * m + 16 * (c >> 2) + (c & 3);
        adA[0] = row * 128 + ((g ^ (c & 7)) << 4);
        adA[1] = adA[0] ^ 64;
        if (SSM) {
          adB[0] = 16384 + (16 * n + c) * 128 + ((g ^ (c & 7)) << 4);
          adB[1] = adB[0] ^ 64;
        } else {
          const int q = c >> 2, pp = c & 3, key = ((q >> 1) & 1) | ((g & 1) << 1);
#pragma unroll
          for (int u = 0; u < 3; ++u) {
            const int ct = 3 * n + u;
            adB[u] = 16384 + (ct >> 2) * 8192 + (8 * g + q) * 128 + (((((ct & 3) ^ key) << 1) | (pp >> 1)) << 4) + 8 * (pp & 1);
          }
        }
      }
      f4 acc[4][NT], asum[NT];
      const __bf16 one = (__bf16)1.0f;
      const bf8 ones = {one, one, one, one, one, one, one, one};
#pragma unroll
      for (int u = 0; u < NT; ++u) asum[u] = (f4){0.f, 0.f, 0.f, 0.f};
#pragma unroll
      for (int a = 0; a < 4; ++a)
#pragma unroll
        for (int u = 0; u < NT; ++u) acc[a][u] = (f4){0.f, 0.f, 0.f, 0.f};
      bf8 A1[4], B1[NT];
      auto frags = [&](unsigned slot, int ks, bf8* A, bf8* B) {
#pragma unroll
        for (int a = 0; a < 4; ++a) A[a] = lds_b128(slot + adA[ks] + 512 * a);
        if (SSM) {
          B[0] = lds_b128(slot + adB[ks]);
          B[1] = lds_b128(slot + adB[ks] + 8192);
        } else {
#pragma unroll
          for (int u = 0; u < 3; ++u) B[u] = lds_tr(slot + adB[u] + ks * 4096);
        }
      };
      auto mm = [&](const bf8* A, const bf8* B) {
#pragma unroll
        for (int a = 0; a < 4; ++a)
#pragma unroll
          for (int u = 0; u < NT; ++u) acc[a][u] = mfma(A[a], B[u], acc[a][u]);
      };
#pragma unroll
      for (int s = 0; s < 8; ++s) {
        LDS_SYNC();
        STAMP(1 + (s >> 1));
        if (s == 0) {
          const unsigned tag = *(LDS const unsigned*)(size_t)C_TAG;
          if (lane == 0) st_gran(p.ws + WS_ROW, t0 + 8 * jj + w, tag, rowv);
        }
        const unsigned slot = (unsigned)(s % NSLOT) * SLOT;
        bf8 A0[4], B0[NT];
        frags(slot, 0, A0, B0);
        SCHED_FENCE();
        if (s > 0) mm(A1, B1);
        SCHED_FENCE();
#pragma unroll
        for (int u = 0; u < NT; ++u)
          if (s > 0) asum[u] = mfma(ones, m ? B1[u] : B0[u], asum[u]);
          else if (m == 0) asum[u] = mfma(ones, B0[u], asum[u]);
        SCHED_FENCE();
        frags(slot, 1, A1, B1);
        SCHED_FENCE();
        mm(A0, B0);
        SCHED_FENCE();
      }
      mm(A1, B1);
      if (m) {
#pragma unroll
        for (int u = 0; u < NT; ++u) asum[u] = mfma(ones, B1[u], asum[u]);
      }
      if (g == 0) {
#pragma unroll
        for (int u = 0; u < NT; ++u) lds_st(C_CS + (m * 192 + (NT * n + u) * 16 + c) * 4, asum[u][0]);
      }
      LDS_SYNC();
      STAMP(5);
      float ncs[NT];
#pragma unroll
      for (int u = 0; u < NT; ++u)
        ncs[u] = -(lds_ld(C_CS + ((NT * n + u) * 16 + c) * 4) + lds_ld(C_CS + (192 + (NT * n + u) * 16 + c) * 4));
      char* ht = p.ws + WS_H + (size_t)(2 * rt + m) * KH * 128;
      const unsigned amr = (64 * m + 16 * g) * 4;
      if (!SSM) {
        unsigned o[3][8];
#pragma unroll
        for (int a = 0; a < 4; ++a) {
          const f4 mean4 = lds_ld4(C_MEAN + amr + 16 * a), rinv4 = lds_ld4(C_RINV + amr + 16 * a);
          f2 v[6];
#pragma unroll
          for (int q = 0; q < 6; ++q) {
            const int u = q >> 1, i = q & 1;
            v[q] = __builtin_elementwise_fma((f2){mean4[2 * i], mean4[2 * i + 1]}, (f2){ncs[u], ncs[u]}, (f2){acc[a][u][2 * i], acc[a][u][2 * i + 1]});
          }
          SCHED_FENCE();
#pragma unroll
          for (int q = 0; q < 6; ++q) v[q] *= (f2){rinv4[2 * (q & 1)], rinv4[2 * (q & 1) + 1]};
          SCHED_FENCE();
          unsigned pk[6];
#pragma unroll
          for (int q = 0; q < 6; ++q) pk[q] = pack2(v[q].x, v[q].y);
          SCHED_FENCE();
#pragma unroll
          for (int q = 0; q < 6; ++q)
            o[q >> 1][2 * a + (q & 1)] = __builtin_bit_cast(unsigned, __builtin_elementwise_max(__builtin_bit_cast(s2, pk[q]), (s2){0, 0}));
          SCHED_FENCE();
        }
#pragma unroll
        for (int u = 0; u < 3; ++u) {
          const int f0 = f_ld + 16 * (3 * n + u);
          if (f0 >= f_lo && f0 < f_hi)
            store_tile(ht, 1024 + f0, c, g, (u4){o[u][0], o[u][1], o[u][2], o[u][3]}, (u4){o[u][4], o[u][5], o[u][6], o[u][7]});
        }
      } else {
        const int chl = 16 * n + c, seg = 4 * m + g;
        const f2 lam = lds_ld2(C_LAM + chl * 8), cc = lds_ld2(C_LAM + 512 + chl * 8);
        f2 st = {0.f, 0.f};
#pragma unroll
        for (int a = 0; a < 4; ++a) {
          const f4 mean4 = lds_ld4(C_MEAN + amr + 16 * a), rinv4 = lds_ld4(C_RINV + amr + 16 * a);
          f2 vr[2], vi[2], kx[2], ky[2], tr[2], ti[2];
#pragma unroll
          for (int i = 0; i < 2; ++i) {
            const f2 mn = {mean4[2 * i], mean4[2 * i + 1]}, ri = {rinv4[2 * i], rinv4[2 * i + 1]};
            vr[i] = __builtin_elementwise_fma(mn, (f2){ncs[0], ncs[0]}, (f2){acc[a][0][2 * i], acc[a][0][2 * i + 1]});
            vi[i] = __builtin_elementwise_fma(mn, (f2){ncs[1], ncs[1]}, (f2){acc[a][1][2 * i], acc[a][1][2 * i + 1]});
            kx[i] = ri * cc.x; ky[i] = ri * cc.y;
          }
          SCHED_FENCE();
#pragma unroll
          for (int i = 0; i < 2; ++i) { tr[i] = ky[i] * vi[i]; ti[i] = ky[i] * vr[i]; }
          SCHED_FENCE();
#pragma unroll
          for (int i = 0; i < 2; ++i) {
            const f2 br = __builtin_elementwise_fma(kx[i], vr[i], -tr[i]);
            const f2 bi = __builtin_elementwise_fma(-kx[i], vi[i], -ti[i]);
            acc[a][0][2 * i] = br.x; acc[a][0][2 * i + 1] = br.y; acc[a][1][2 * i] = bi.x; acc[a][1][2 * i + 1] = bi.y;
          }
          SCHED_FENCE();
        }
#pragma unroll
        for (int a = 0; a < 4; ++a)
#pragma unroll
          for (int i = 0; i < 4; ++i) st = cmul_add(lam, st, (f2){acc[a][0][i], acc[a][1][i]});
        lds_st2(C_AGG + (seg * 64 + chl) * 8, st);
        LDS_SYNC();
        STAMP(6);
        LDS_SYNC();
        STAMP(7);
        f2 h = lds_ld2(C_CIN + (seg * 64 + chl) * 8);
        u4 ore[2], oim[2];
#pragma unroll
        for (int a = 0; a < 4; ++a) {
          float hr[4], hi[4];
#pragma unroll
          for (int i = 0; i < 4; ++i) {
            h = cmul_add(lam, h, (f2){acc[a][0][i], acc[a][1][i]});
            hr[i] = h.x; hi[i] = h.y;
          }
          ore[a >> 1][2 * (a & 1)] = pack2(hr[0], hr[1]); ore[a >> 1][2 * (a & 1) + 1] = pack2(hr[2], hr[3]);
          oim[a >> 1][2 * (a & 1)] = pack2(hi[0], hi[1]); oim[a >> 1][2 * (a & 1) + 1] = pack2(hi[2], hi[3]);
        }
        store_tile(ht, j * 64 + 16 * n, c, g, ore[0], ore[1]);
        store_tile(ht, 512 + j * 64 + 16 * n, c, g, oim[0], oim[1]);
      }
    }
    STAMP(8);
    if (!SSM) {
      asm volatile("s_waitcnt vmcnt(0)" ::: "memory");
      BARRIER();
    }
    STAMP(9);

    const int kq = w & 3, th = w >> 2;
    unsigned adT[4], adN;
    {
      const int q = c >> 2, pp = c & 3, key = ((q >> 1) & 1) | ((g & 1) << 1);
#pragma unroll
      for (int u = 0; u < 4; ++u) adT[u] = (32 * kq + 8 * g + q) * 128 + ((((u ^ key) << 1) | (pp >> 1)) << 4) + 8 * (pp & 1);
      adN = (kq >> 1) * 8192 + c * 128 + (((4 * (kq & 1) + g) ^ (c & 7)) << 4);
    }
    const unsigned adH0 = 16384 + (th ? adT[2] : adT[0]), adH1 = 16384 + (th ? adT[3] : adT[1]);
    f4 ac2[4][2];
#pragma unroll
    for (int a = 0; a < 4; ++a) { ac2[a][0] = (f4){0.f, 0.f, 0.f, 0.f}; ac2[a][1] = (f4){0.f, 0.f, 0.f, 0.f}; }
    for (int s = 0; s < 19; ++s) {
      if (SSM && s == DRAIN_S) asm volatile("s_waitcnt vmcnt(0)" ::: "memory");
      LDS_SYNC();
      const unsigned slot = (unsigned)(s & 3) * 32768;
      if (s == 10 && kq == 3) continue;
      bf8 W[4];
      if (s >= 11) {
#pragma unroll
        for (int a = 0; a < 4; ++a) W[a] = lds_b128(slot + adN + a * 2048);
      } else {
#pragma unroll
        for (int a = 0; a < 4; ++a) W[a] = lds_tr(slot + adT[a]);
      }
      const bf8 H0 = lds_tr(slot + adH0), H1 = lds_tr(slot + adH1);
#pragma unroll
      for (int a = 0; a < 4; ++a) { ac2[a][0] = mfma(W[a], H0, ac2[a][0]); ac2[a][1] = mfma(W[a], H1, ac2[a][1]); }
    }
    LDS_SYNC();
#pragma unroll
    for (int a = 0; a < 4; ++a)
#pragma unroll
      for (int u = 0; u < 2; ++u) {
        const int t = 32 * th + 16 * u + c;
        lds_st4(kq * 16384 + t * 256 + (((4 * a + g) ^ (t & 7)) << 4), ac2[a][u]);
      }
  }
  u2 xr[2], dr;
  if (w < 8) {
    const unsigned ox = (unsigned)((8 * w + g) * D + d0 + 4 * c) * 2, od = (unsigned)(d0 + 4 * c) * 2;
    const char* xb = (const char*)(p.x + (size_t)tt * 64 * D);
    xr[0] = *(const u2*)(xb + ox); xr[1] = *(const u2*)(xb + 4 * D * 2 + ox);
    dr = *(const u2*)((const char*)p.dsk + od);
  }
  LDS_SYNC();
  if (w < 8) {
    const f4 dsk = {__uint_as_float(dr.x << 16), __uint_as_float(dr.x & 0xffff0000u), __uint_as_float(dr.y << 16), __uint_as_float(dr.y & 0xffff0000u)};
#pragma unroll
    for (int r = 0; r < 2; ++r) {
      const int t = 8 * w + 4 * r + g;
      const unsigned a = t * 256 + ((c ^ (t & 7)) << 4);
      f4 v = lds_ld4(a) + lds_ld4(a + 16384) + lds_ld4(a + 32768) + lds_ld4(a + 49152);
      const int tl = hf * 64 + t;
      const float mb = lds_ld(C_MEAN + tl * 4), ri = lds_ld(C_RINV + tl * 4);
      const float xv[4] = {__uint_as_float(xr[r].x << 16), __uint_as_float(xr[r].x & 0xffff0000u),
                           __uint_as_float(xr[r].y << 16), __uint_as_float(xr[r].y & 0xffff0000u)};
#pragma unroll
      for (int i = 0; i < 4; ++i) {
        const float xn = bfr(bfr(xv[i] - mb) * ri);
        v[i] = xv[i] + (v[i] + bfr(xn * dsk[i]));
      }
      *(f4*)(p.out + (size_t)(tt * 64 + t) * D + d0 + 4 * c) = v;
    }
  }
  STAMP(14);
  STAMP(15);
  STAMP_OUT(0);
}

__global__ __launch_bounds__(768, 3) void s5_fused(Params p) {
  const int b = blockIdx.x, q = b & 7, idx = b >> 3;
  const int rt = 4 * (q >> 1) + (idx >> 3), i8 = idx & 7, j = 4 * (q & 1) + (i8 & 3);
  if (i8 < 4) body<true>(p, rt, j, i8);
  else body<false>(p, rt, j, i8);
}

extern "C" void kernel_launch(void* const* d_in, const int* in_sizes, int n_in, void* d_out, int out_size,
                              void* d_ws, size_t ws_size, hipStream_t stream) {
  Params p;
  p.x = (const u16*)d_in[0]; p.lre = (const u16*)d_in[1]; p.lim = (const u16*)d_in[2];
  p.bre = (const u16*)d_in[3]; p.bim = (const u16*)d_in[4]; p.cre = (const u16*)d_in[5]; p.cim = (const u16*)d_in[6];
  p.ldt = (const u16*)d_in[7]; p.dsk = (const u16*)d_in[8]; p.w1 = (const u16*)d_in[10]; p.w2 = (const u16*)d_in[11];
  p.out = (float*)d_out; p.ws = (char*)d_ws;
  (void)hipFuncSetAttribute((const void*)s5_fused, hipFuncAttributeMaxDynamicSharedMemorySize, LDS_BYTES);
  s5_fused<<<256, 768, LDS_BYTES, stream>>>(p);
}
```

```cpp
#define TLBT
#include <hip/hip_runtime.h>
#include <hip/hip_bf16.h>
#include <stdint.h>

typedef __attribute__((ext_vector_type(8))) __bf16 bf8;
typedef __attribute__((ext_vector_type(4))) __bf16 bf4;
typedef __attribute__((ext_vector_type(2))) __bf16 bf2;
typedef __attribute__((ext_vector_type(4))) float f4;
typedef __attribute__((ext_vector_type(2))) float f2;
typedef __attribute__((ext_vector_type(4))) unsigned u4;
typedef __attribute__((ext_vector_type(2))) unsigned u2;
typedef __attribute__((ext_vector_type(2))) short s2;
typedef unsigned long long u64;
typedef unsigned short u16;
#define LDS __attribute__((address_space(3)))
#define GLB __attribute__((address_space(1)))

constexpr int T = 2048, D = 512, P = 512, F = 1376;
constexpr int KH = 2432;
constexpr size_t WS_EPOCH = 0;
constexpr size_t WS_FLAG = 4096;
constexpr size_t WS_ROW = 8192;
constexpr size_t WS_REC = 65536;
constexpr size_t WS_STAMP = 128u << 20;
#define STAMP_DECL
#define STAMP(i)
#define STAMP_OUT(k)
constexpr size_t WS_H = 1 << 20;

__device__ __forceinline__ void glds_x4(const unsigned* vo, const char* sbase, unsigned lds_addr) {
  unsigned keep;
  asm volatile("s_mov_b32 %0, m0\n\ts_mov_b32 m0, %6\n\ts_nop 0\n\tglobal_load_lds_dwordx4 %1, %5\n\t"
               "global_load_lds_dwordx4 %2, %5 offset:1024\n\tglobal_load_lds_dwordx4 %3, %5 offset:2048\n\t"
               "global_load_lds_dwordx4 %4, %5 offset:3072\n\ts_mov_b32 m0, %0"
               : "=&s"(keep) : "v"(vo[0]), "v"(vo[1]), "v"(vo[2]), "v"(vo[3]), "s"(sbase - 4096), "s"(lds_addr));
}
__device__ __forceinline__ void glds_x4_dev(const unsigned* vo, const char* sbase, unsigned lds_addr) {
  unsigned keep;
  asm volatile("s_mov_b32 %0, m0\n\ts_mov_b32 m0, %6\n\ts_nop 0\n\tglobal_load_lds_dwordx4 %1, %5 sc1\n\t"
               "global_load_lds_dwordx4 %2, %5 offset:1024 sc1\n\tglobal_load_lds_dwordx4 %3, %5 offset:2048 sc1\n\t"
               "global_load_lds_dwordx4 %4, %5 offset:3072 sc1\n\ts_mov_b32 m0, %0"
               : "=&s"(keep) : "v"(vo[0]), "v"(vo[1]), "v"(vo[2]), "v"(vo[3]), "s"(sbase - 4096), "s"(lds_addr));
}
__device__ __forceinline__ void glds_x2(const unsigned* vo, const char* sbase, unsigned lds_addr) {
  unsigned keep;
  asm volatile("s_mov_b32 %0, m0\n\ts_mov_b32 m0, %4\n\ts_nop 0\n\tglobal_load_lds_dwordx4 %1, %3\n\t"
               "global_load_lds_dwordx4 %2, %3 offset:1024\n\ts_mov_b32 m0, %0"
               : "=&s"(keep) : "v"(vo[0]), "v"(vo[1]), "s"(sbase - 4096), "s"(lds_addr));
}
__device__ __forceinline__ void touch(unsigned voff, const void* sbase, unsigned lds_addr) {
  unsigned keep;
  asm volatile("s_mov_b32 %0, m0\n\ts_mov_b32 m0, %3\n\ts_nop 0\n\tglobal_load_lds_dword %1, %2\n\ts_mov_b32 m0, %0"
               : "=&s"(keep) : "v"(voff), "s"(sbase), "s"(lds_addr));
}
constexpr int NPF = 3;
template <int N> __device__ __forceinline__ void wait_vm() { asm volatile("s_waitcnt vmcnt(%0) lgkmcnt(0)" :: "n"(N) : "memory"); }
#define WAIT_VM(n) asm volatile("s_waitcnt vmcnt(" #n ") lgkmcnt(0)" ::: "memory")
#define BARRIER() do { __builtin_amdgcn_s_barrier(); asm volatile("" ::: "memory"); } while (0)
#define SCHED_FENCE() __builtin_amdgcn_sched_barrier(0)
#define LDS_SYNC() do { asm volatile("s_waitcnt lgkmcnt(0)" ::: "memory"); BARRIER(); } while (0)

__device__ __forceinline__ bf8 lds_b128(unsigned a) { return *(LDS const bf8*)(size_t)a; }
__device__ __forceinline__ bf8 lds_tr(unsigned a) {
  bf4 lo = __builtin_amdgcn_ds_read_tr16_b64_v4bf16((LDS bf4*)(size_t)a);
  bf4 hi = __builtin_amdgcn_ds_read_tr16_b64_v4bf16((LDS bf4*)(size_t)(a + 512));
  return __builtin_shufflevector(lo, hi, 0, 1, 2, 3, 4, 5, 6, 7);
}
__device__ __forceinline__ f4 mfma(bf8 a, bf8 b, f4 c) { return __builtin_amdgcn_mfma_f32_16x16x32_bf16(a, b, c, 0, 0, 0); }
__device__ __forceinline__ float bfr(float v) { return (float)(__bf16)v; }
__device__ __forceinline__ float bf2f(u16 v) { return __uint_as_float((unsigned)v << 16); }
__device__ __forceinline__ unsigned pack2(float a, float b) { return __builtin_bit_cast(unsigned, __builtin_convertvector((f2){a, b}, bf2)); }
__device__ __forceinline__ void lds_st(unsigned a, float v) { *(LDS float*)(size_t)a = v; }
__device__ __forceinline__ float lds_ld(unsigned a) { return *(LDS const float*)(size_t)a; }
__device__ __forceinline__ f4 lds_ld4(unsigned a) { return *(LDS const f4*)(size_t)a; }
__device__ __forceinline__ f2 lds_ld2(unsigned a) { return *(LDS const f2*)(size_t)a; }
__device__ __forceinline__ void lds_st2(unsigned a, f2 v) { *(LDS f2*)(size_t)a = v; }
__device__ __forceinline__ void lds_st4(unsigned a, f4 v) { *(LDS f4*)(size_t)a = v; }
__device__ __forceinline__ f2 cmul_add(f2 a, f2 b, f2 c) {
  return (f2){fmaf(-a.y, b.y, fmaf(a.x, b.x, c.x)), fmaf(a.y, b.x, fmaf(a.x, b.y, c.y))};
}
__device__ __forceinline__ f2 cmul(f2 a, f2 b) { return (f2){a.x * b.x - a.y * b.y, a.x * b.y + a.y * b.x}; }

struct Params {
  const u16* x; const u16* lre; const u16* lim; const u16* bre; const u16* bim; const u16* cre; const u16* cim;
  const u16* ldt; const u16* dsk; const u16* w1; const u16* w2; float* out; char* ws;
};

constexpr unsigned C_LAM = 131072;
constexpr unsigned C_MEAN = C_LAM + 1024;
constexpr unsigned C_RINV = C_MEAN + 512;
constexpr unsigned C_CS = C_RINV + 512;
constexpr unsigned C_TAG = C_CS + 1536;
constexpr unsigned C_AGG = C_TAG + 256;
constexpr unsigned C_CIN = C_AGG + 4096;
constexpr unsigned C_JUNK = C_CIN + 4096;
constexpr unsigned LDS_BYTES = C_JUNK + 256;
constexpr int SPIN_MAX = 400000;
constexpr int DRAIN_S = 2;

template <int CTRL> __device__ __forceinline__ float dpp_add(float v) {
  return v + __int_as_float(__builtin_amdgcn_update_dpp(0, __float_as_int(v), CTRL, 0xf, 0xf, true));
}
__device__ __forceinline__ float wave_sum(float v) {
  v = dpp_add<0x111>(v); v = dpp_add<0x112>(v); v = dpp_add<0x114>(v); v = dpp_add<0x118>(v);
  v += __int_as_float(__builtin_amdgcn_update_dpp(0, __float_as_int(v), 0x142, 0xa, 0xf, true));
  v += __int_as_float(__builtin_amdgcn_update_dpp(0, __float_as_int(v), 0x143, 0xc, 0xf, true));
  return __int_as_float(__builtin_amdgcn_readlane(__float_as_int(v), 63));
}
__device__ __forceinline__ void st_gran(char* base, unsigned idx, unsigned tag, unsigned val) {
  __hip_atomic_store((GLB u64*)base + idx, ((u64)tag << 32) | val, __ATOMIC_RELAXED, __HIP_MEMORY_SCOPE_AGENT);
}
__device__ __forceinline__ u64 ld_gran(char* base, unsigned idx) {
  return __hip_atomic_load((GLB u64*)base + idx, __ATOMIC_RELAXED, __HIP_MEMORY_SCOPE_AGENT);
}
__device__ __forceinline__ unsigned wait_gran(char* base, unsigned idx, unsigned tag, u64 v) {
  if (!__all((unsigned)(v >> 32) == tag)) {
    int spin = 0;
    do {
      __builtin_amdgcn_s_sleep(1);
      v = ld_gran(base, idx);
    } while (!__all((unsigned)(v >> 32) == tag) && ++spin < SPIN_MAX);
  }
  return (unsigned)v;
}
__device__ __forceinline__ void st16_dev(char* base, unsigned off, u4 v) {
  asm volatile("global_store_dwordx4 %0, %1, %2 sc1\n\ts_nop 1" :: "v"(off), "v"(v), "s"(base) : "memory");
}
__device__ __forceinline__ unsigned ror8(unsigned v) { return (unsigned)__builtin_amdgcn_update_dpp(0, (int)v, 0x128, 0xf, 0xf, true); }
__device__ __forceinline__ void store_tile(char* ht, unsigned k0, int c, int g, u4 lo, u4 hi) {
  const bool low = c < 8;
  u4 a, b;
#pragma unroll
  for (int i = 0; i < 4; ++i) {
    const unsigned wv = ror8(low ? hi[i] : lo[i]);
    a[i] = low ? lo[i] : wv; b[i] = low ? wv : hi[i];
  }
  const unsigned off = (k0 + (c & 7)) * 128 + (2 * g + (c >> 3)) * 16;
  st16_dev(ht, off, a);
  st16_dev(ht, off + 1024, b);
}
__device__ __forceinline__ void unpack8(u4 v, float* f) {
#pragma unroll
  for (int i = 0; i < 4; ++i) { f[2 * i] = __uint_as_float(v[i] << 16); f[2 * i + 1] = __uint_as_float(v[i] & 0xffff0000u); }
}

template <bool SSM>
__device__ __forceinline__ void body(const Params& p, int rt, int j, int i8) {
  constexpr int NT = SSM ? 2 : 3;
  constexpr int NSLOT = SSM ? 4 : 3;
  constexpr unsigned SLOT = SSM ? 32768 : 40960;
  const int tid = threadIdx.x, lane = tid & 63;
  const int w = __builtin_amdgcn_readfirstlane(tid >> 6);
  const int c = lane & 15, g = lane >> 4;
  const int t0 = rt * 128;
  const int jj = SSM ? j : j + 8;
  const int hf = j >> 2, r4 = rt & 3;
  const int tt = 2 * rt + hf, d0 = i8 * 64;
  STAMP_DECL; STAMP(0);
  int f_lo = 0, f_hi = 0, f_ld = 0;
  if (!SSM) { f_lo = 16 * ((86 * j) >> 3); f_hi = 16 * ((86 * (j + 1)) >> 3); f_ld = f_lo < F - 192 ? f_lo : F - 192; }

  if (w >= 8) {
    const int L = w - 8;
    unsigned r_lre = 0, r_lim = 0, r_ldt = 0;
    if (SSM && L == 0) {
      const unsigned o = (unsigned)(j * 64 + lane) * 2;
      asm volatile("global_load_ushort %0, %3, %4\n\tglobal_load_ushort %1, %3, %5\n\tglobal_load_ushort %2, %3, %6"
                   : "=&v"(r_lre), "=&v"(r_lim), "=&v"(r_ldt) : "v"(o), "s"(p.lre), "s"(p.lim), "s"(p.ldt));
    }
    unsigned voA[4], voB[6];
#pragma unroll
    for (int u = 0; u < 4; ++u) {
      const int row = 32 * L + 8 * u + (lane >> 3), key = (row & 3) | ((row >> 2) & 4);
      voA[u] = (unsigned)(row * D + (((lane & 7) ^ key) << 3)) * 2 + 4096 - 1024 * u;
    }
    const char* baseA = (const char*)(p.x + (size_t)t0 * D);
    const char* baseB;
    if (SSM) {
#pragma unroll
      for (int u = 0; u < 4; ++u) {
        const int row = (32 * L + 8 * u + (lane >> 3)) & 63;
        voB[u] = (unsigned)(row * D + (((lane & 7) ^ (row & 7)) << 3)) * 2 + 4096 - 1024 * u;
      }
      baseB = (const char*)((L < 2 ? p.bre : p.bim) + (size_t)j * 64 * D);
    } else {
#pragma unroll
      for (int u = 0; u < 6; ++u) {
        const int i = 6 * L + u, k = 8 * (i & 7) + (lane >> 3), key = ((k >> 1) & 1) | ((k >> 2) & 2);
        voB[u] = (unsigned)(k * F + 64 * (i >> 3) + (((lane & 7) ^ (key << 1)) << 3)) * 2 + 4096 - 1024 * (u & 3);
      }
      baseB = (const char*)(p.w1 + f_ld);
    }
    auto stage = [&](int s) {
      const int ks = s;
      const unsigned slot = (unsigned)(s % NSLOT) * SLOT;
      glds_x4(voA, baseA + ks * 128, slot + L * 4096);
      if (SSM) {
        glds_x4(voB, baseB + ks * 128, slot + 16384 + L * 4096);
      } else {
        const char* b = baseB + (size_t)ks * 64 * F * 2;
        glds_x4(voB, b, slot + 16384 + L * 6144);
        glds_x2(voB + 4, b, slot + 16384 + L * 6144 + 4096);
      }
    };
#pragma unroll
    for (int s = 0; s < NSLOT - 1; ++s) stage(s);
    char* const gbase = p.ws + WS_ROW;
    const unsigned gi0 = t0 + 64 * (L & 1) + lane;
    u64 g0 = 0;
    unsigned tag = 0;
#pragma unroll
    for (int s = 0; s < 8; ++s) {
      if (SSM) { if (s < 5) WAIT_VM(16); else if (s == 5) wait_vm<17 + NPF>(); else if (s == 6) wait_vm<9 + NPF>(); else wait_vm<NPF>(); }
      else     { if (s < 6) WAIT_VM(10); else if (s == 6) wait_vm<10 + NPF>(); else wait_vm<NPF>(); }
      BARRIER();
      if (s == 4) asm volatile("global_load_dwordx2 %0, %1, %2 sc1" : "=&v"(g0) : "v"(gi0 * 8), "s"(gbase));
      if (s + NSLOT - 1 < 8) stage(s + NSLOT - 1);
      if (s + NSLOT - 1 == 7) {
        const unsigned sh = r4 * 8 + i8;
        touch((sh * 128 + (L & 1) * 64 + lane) * 128, L < 2 ? p.cre : p.cim, C_JUNK);
        touch((sh * 344 + L * 86 + lane) * 128, p.w2, C_JUNK);
        touch((sh * 344 + L * 86 + 64 + (lane < 22 ? lane : 21)) * 128, p.w2, C_JUNK);
      }
      if (s == 0) {
        tag = *(LDS const unsigned*)(size_t)C_TAG;
        if (SSM && L == 0) {
          asm volatile("" : "+v"(r_lre), "+v"(r_lim), "+v"(r_ldt));
          const float lr = bf2f((u16)r_lre), li = bf2f((u16)r_lim);
          const float dt = bfr(expf(bf2f((u16)r_ldt)));
          const float e = expf(dt * lr);
          float sn, cn; sincosf(dt * li, &sn, &cn);
          const f2 lam = {e * cn, e * sn};
          const float nr = lam.x - 1.0f, ni = lam.y, den = lr * lr + li * li;
          const float inv = 2.0f / den;
          float n0 = fmaf(nr, lr, ni * li), n1 = fmaf(ni, lr, -nr * li);
          asm volatile("" : "+v"(n0), "+v"(n1));
          lds_st2(C_LAM + lane * 8, (f2){lam.x, -lam.y});
          lds_st2(C_LAM + 512 + lane * 8, (f2){n0 * inv, n1 * inv});
        }
      }
      if (s == 7) {
        asm volatile("" : "+v"(g0));
        if (L < 2) {
          const unsigned v0 = wait_gran(gbase, gi0, tag, g0);
          lds_st(C_MEAN + (64 * L + lane) * 4, __uint_as_float(v0 & 0xffff0000u));
          lds_st(C_RINV + (64 * L + lane) * 4, 1.0f / __uint_as_float(v0 << 16));
        }
      }
    }
    LDS_SYNC();

    unsigned voH[4], voN[4], voK[4];
#pragma unroll
    for (int u = 0; u < 4; ++u) {
      const int i = 4 * L + u, r8 = lane >> 3, pos = lane & 7;
      const int k = 8 * i + r8, key = ((k >> 1) & 1) | ((k >> 2) & 2);
      const int chunk = pos ^ (key << 1);
      voH[u] = (unsigned)(k * 128 + chunk * 16) + 4096 - 1024 * u;
      voK[u] = (unsigned)(k * (D * 2) + chunk * 16) + 4096 - 1024 * u;
      const int row = 8 * (i & 7) + r8;
      voN[u] = (unsigned)(row * (P * 2) + (i >> 3) * 128 + ((pos ^ (row & 7)) << 4)) + 4096 - 1024 * u;
    }
    const char* hbase = p.ws + WS_H + (size_t)tt * KH * 128;
    auto stageW = [&](int s) {
      const unsigned slot = (unsigned)(s & 3) * 32768;
      if (s >= 11) {
        const int st = s - 11;
        glds_x4(voN, (const char*)((st < 4 ? p.cre : p.cim) + (size_t)d0 * P + (st & 3) * 128), slot + L * 4096);
      } else {
        const char* wb = (const char*)(p.w2 + (size_t)s * 128 * D + d0);
        if (s == 10 && L == 3) wb -= 32 * D * 2;
        glds_x4(voK, wb, slot + L * 4096);
      }
    };
    auto stageH = [&](int s) {
      const int kb = s >= 11 ? s - 11 : s + 8;
      glds_x4_dev(voH, hbase + (size_t)kb * 16384, (unsigned)(s & 3) * 32768 + 16384 + L * 4096);
    };
    if (!(SSM && L == 0)) { stageW(0); stageW(1); stageW(2); }
    if (SSM) {
      f2 l16 = {0.f, 0.f}, l128 = {0.f, 0.f};
      if (L == 0) {
        l16 = lds_ld2(C_LAM + lane * 8);
#pragma unroll
        for (int i = 0; i < 4; ++i) l16 = cmul(l16, l16);
        l128 = l16;
#pragma unroll
        for (int i = 0; i < 3; ++i) l128 = cmul(l128, l128);
      }
      LDS_SYNC();
      if (L == 0) {
        f2 ag[8];
#pragma unroll
        for (int i = 0; i < 8; ++i) ag[i] = lds_ld2(C_AGG + (i * 64 + lane) * 8);
        f2 tot = ag[0];
#pragma unroll
        for (int i = 1; i < 8; ++i) tot = cmul_add(l16, tot, ag[i]);
        char* rec = p.ws + WS_REC + (size_t)j * 1024;
        if (rt < 15) {
          st_gran(rec, rt * 1024 + lane, tag, __float_as_uint(tot.x));
          st_gran(rec, rt * 1024 + 64 + lane, tag, __float_as_uint(tot.y));
        }
        f2 carry = {0.f, 0.f};
        if (rt > 0) {
          u64 vr[15], vi[15];
          for (int spin = 0; spin < SPIN_MAX; ++spin) {
            bool ok = true;
#pragma unroll
            for (int i = 0; i < 15; ++i) {
              const int ii = i < rt ? i : 0;
              vr[i] = ld_gran(rec, ii * 1024 + lane);
              vi[i] = ld_gran(rec, ii * 1024 + 64 + lane);
            }
#pragma unroll
            for (int i = 0; i < 15; ++i) ok = ok && (unsigned)(vr[i] >> 32) == tag && (unsigned)(vi[i] >> 32) == tag;
            if (__all(ok)) break;
            __builtin_amdgcn_s_sleep(1);
          }
#pragma unroll
          for (int i = 0; i < 15; ++i)
            if (i < rt) carry = cmul_add(l128, carry, (f2){__uint_as_float((unsigned)vr[i]), __uint_as_float((unsigned)vi[i])});
        }
#pragma unroll
        for (int i = 0; i < 8; ++i) {
          lds_st2(C_CIN + (i * 64 + lane) * 8, carry);
          carry = cmul_add(l16, carry, ag[i]);
        }
      }
      LDS_SYNC();
      if (L == 0) { stageW(0); stageW(1); stageW(2); }
    }
    char* const flags = p.ws + WS_FLAG;
    if (!SSM) {
      BARRIER();
      if (L == 0 && lane == 0) st_gran(flags, rt * 16 + jj, tag, 0);
    }
    {
      const unsigned fi = rt * 16 + 8 + (lane & 7);
      u64 v;
      asm volatile("global_load_dwordx2 %0, %1, %2 sc1\n\ts_waitcnt vmcnt(0)" : "=&v"(v) : "v"(fi * 8), "s"(flags) : "memory");
      wait_gran(flags, fi, tag, v);
      asm volatile("s_waitcnt vmcnt(0)" ::: "memory");
    }
    stageH(0); stageH(1); stageH(2);
    u64 fl;
#pragma unroll
    for (int s = 0; s < 19; ++s) {
      if (s == 0) WAIT_VM(8); else if (s == 1) WAIT_VM(12);
      else if (s == 6 || s == 7) WAIT_VM(17);
      else if (s < 17) WAIT_VM(16); else if (s == 17) WAIT_VM(8); else WAIT_VM(0);
      BARRIER();
      if (s == 8) {
        asm volatile("" : "+v"(fl));
        wait_gran(flags, rt * 16 + (lane & 7), tag, fl);
      }
      if (s + 3 < 19) { stageW(s + 3); stageH(s + 3); }
      if (SSM && s == DRAIN_S && L == 0 && lane == 0) st_gran(flags, rt * 16 + jj, tag, 0);
      if (s == 5) asm volatile("global_load_dwordx2 %0, %1, %2 sc1" : "=&v"(fl) : "v"((rt * 16 + (lane & 7)) * 8), "s"(flags));
    }
    if (L == 1 && rt == 0 && jj == 0) {
#pragma unroll
      for (int i = 0; i < 4; ++i) wait_gran(p.ws + WS_ROW, (i * 64 + lane) * 8, tag, ld_gran(p.ws + WS_ROW, (i * 64 + lane) * 8));
      if (lane == 0) __hip_atomic_store((GLB unsigned*)(p.ws + WS_EPOCH), tag, __ATOMIC_RELAXED, __HIP_MEMORY_SCOPE_AGENT);
    }
    LDS_SYNC();
  } else {
    const int m = w >> 2, n = w & 3;
    unsigned rowv;
    {
      u4 xv;
      unsigned ep;
      const unsigned xo = (unsigned)((8 * jj + w) * D + lane * 8) * 2, z = 0;
      asm volatile("global_load_dwordx4 %0, %2, %4\n\tglobal_load_dword %1, %3, %5 sc1\n\ts_waitcnt vmcnt(0)"
                   : "=&v"(xv), "=&v"(ep) : "v"(xo), "v"(z), "s"(p.x + (size_t)t0 * D), "s"(p.ws + WS_EPOCH) : "memory");
      if (w == 0 && lane == 0) *(LDS unsigned*)(size_t)C_TAG = ep + 1u;
      float f[8];
      unpack8(xv, f);
      float sm = 0.f;
#pragma unroll
      for (int i = 0; i < 8; ++i) sm += f[i];
      const float mean = wave_sum(sm) * (1.0f / D);
      float sq = 0.f;
#pragma unroll
      for (int i = 0; i < 8; ++i) { const float dd = f[i] - mean; sq = fmaf(dd, dd, sq); }
      const float var = wave_sum(sq) * (1.0f / D);
      const float mb = bfr(mean), den = bfr(sqrtf(bfr(bfr(var) + 1e-5f)));
      rowv = (__float_as_uint(mb) & 0xffff0000u) | (__float_as_uint(den) >> 16);
      if (w == 7) {
        const char* a = (const char*)p.dsk;
        const char* ob = (const char*)(p.out + (size_t)tt * 64 * D);
        const char* hb = p.ws + WS_H + 2400 * 128;
        if (lane >= 1 && lane < 4) a = ob + (lane - 1) * 65472;
        if (lane == 4) a = hb + (size_t)tt * KH * 128;
        if (lane == 5) a = hb + (size_t)(2 * rt) * KH * 128;
        if (lane == 6) a = hb + (size_t)(2 * rt + 1) * KH * 128;
        unsigned keep;
        asm volatile("s_mov_b32 %0, m0\n\ts_mov_b32 m0, %2\n\ts_nop 0\n\tglobal_load_lds_dword %1, off\n\ts_mov_b32 m0, %0"
                     : "=&s"(keep) : "v"(a), "s"(C_JUNK));
      }
      unsigned adA[2], adB[3];
      {
        const int row = 64 * m + 16 * (c >> 2) + (c & 3);
        adA[0] = row * 128 + ((g ^ (c & 7)) << 4);
        adA[1] = adA[0] ^ 64;
        if (SSM) {
          adB[0] = 16384 + (16 * n + c) * 128 + ((g ^ (c & 7)) << 4);
          adB[1] = adB[0] ^ 64;
        } else {
          const int q = c >> 2, pp = c & 3, key = ((q >> 1) & 1) | ((g & 1) << 1);
#pragma unroll
          for (int u = 0; u < 3; ++u) {
            const int ct = 3 * n + u;
            adB[u] = 16384 + (ct >> 2) * 8192 + (8 * g + q) * 128 + (((((ct & 3) ^ key) << 1) | (pp >> 1)) << 4) + 8 * (pp & 1);
          }
        }
      }
      f4 acc[4][NT], asum[NT];
      const __bf16 one = (__bf16)1.0f;
      const bf8 ones = {one, one, one, one, one, one, one, one};
#pragma unroll
      for (int u = 0; u < NT; ++u) asum[u] = (f4){0.f, 0.f, 0.f, 0.f};
#pragma unroll
      for (int a = 0; a < 4; ++a)
#pragma unroll
        for (int u = 0; u < NT; ++u) acc[a][u] = (f4){0.f, 0.f, 0.f, 0.f};
      bf8 A1[4], B1[NT];
      auto frags = [&](unsigned slot, int ks, bf8* A, bf8* B) {
#pragma unroll
        for (int a = 0; a < 4; ++a) A[a] = lds_b128(slot + adA[ks] + 512 * a);
        if (SSM) {
          B[0] = lds_b128(slot + adB[ks]);
          B[1] = lds_b128(slot + adB[ks] + 8192);
        } else {
#pragma unroll
          for (int u = 0; u < 3; ++u) B[u] = lds_tr(slot + adB[u] + ks * 4096);
        }
      };
      auto mm = [&](const bf8* A, const bf8* B) {
#pragma unroll
        for (int a = 0; a < 4; ++a)
#pragma unroll
          for (int u = 0; u < NT; ++u) acc[a][u] = mfma(A[a], B[u], acc[a][u]);
      };
#pragma unroll
      for (int s = 0; s < 8; ++s) {
        LDS_SYNC();
        STAMP(1 + (s >> 1));
        if (s == 0) {
          const unsigned tag = *(LDS const unsigned*)(size_t)C_TAG;
          if (lane == 0) st_gran(p.ws + WS_ROW, t0 + 8 * jj + w, tag, rowv);
        }
        const unsigned slot = (unsigned)(s % NSLOT) * SLOT;
        bf8 A0[4], B0[NT];
        frags(slot, 0, A0, B0);
        SCHED_FENCE();
        if (s > 0) mm(A1, B1);
        SCHED_FENCE();
#pragma unroll
        for (int u = 0; u < NT; ++u)
          if (s > 0) asum[u] = mfma(ones, m ? B1[u] : B0[u], asum[u]);
          else if (m == 0) asum[u] = mfma(ones, B0[u], asum[u]);
        SCHED_FENCE();
        frags(slot, 1, A1, B1);
        SCHED_FENCE();
        mm(A0, B0);
        SCHED_FENCE();
      }
      mm(A1, B1);
      if (m) {
#pragma unroll
        for (int u = 0; u < NT; ++u) asum[u] = mfma(ones, B1[u], asum[u]);
      }
      if (g == 0) {
#pragma unroll
        for (int u = 0; u < NT; ++u) lds_st(C_CS + (m * 192 + (NT * n + u) * 16 + c) * 4, asum[u][0]);
      }
      LDS_SYNC();
      STAMP(5);
      float ncs[NT];
#pragma unroll
      for (int u = 0; u < NT; ++u)
        ncs[u] = -(lds_ld(C_CS + ((NT * n + u) * 16 + c) * 4) + lds_ld(C_CS + (192 + (NT * n + u) * 16 + c) * 4));
      char* ht = p.ws + WS_H + (size_t)(2 * rt + m) * KH * 128;
      const unsigned amr = (64 * m + 16 * g) * 4;
      if (!SSM) {
        unsigned o[3][8];
#pragma unroll
        for (int a = 0; a < 4; ++a) {
          const f4 mean4 = lds_ld4(C_MEAN + amr + 16 * a), rinv4 = lds_ld4(C_RINV + amr + 16 * a);
          f2 v[6];
#pragma unroll
          for (int q = 0; q < 6; ++q) {
            const int u = q >> 1, i = q & 1;
            v[q] = __builtin_elementwise_fma((f2){mean4[2 * i], mean4[2 * i + 1]}, (f2){ncs[u], ncs[u]}, (f2){acc[a][u][2 * i], acc[a][u][2 * i + 1]});
          }
          SCHED_FENCE();
#pragma unroll
          for (int q = 0; q < 6; ++q) v[q] *= (f2){rinv4[2 * (q & 1)], rinv4[2 * (q & 1) + 1]};
          SCHED_FENCE();
          unsigned pk[6];
#pragma unroll
          for (int q = 0; q < 6; ++q) pk[q] = pack2(v[q].x, v[q].y);
          SCHED_FENCE();
#pragma unroll
          for (int q = 0; q < 6; ++q)
            o[q >> 1][2 * a + (q & 1)] = __builtin_bit_cast(unsigned, __builtin_elementwise_max(__builtin_bit_cast(s2, pk[q]), (s2){0, 0}));
          SCHED_FENCE();
        }
#pragma unroll
        for (int u = 0; u < 3; ++u) {
          const int f0 = f_ld + 16 * (3 * n + u);
          if (f0 >= f_lo && f0 < f_hi)
            store_tile(ht, 1024 + f0, c, g, (u4){o[u][0], o[u][1], o[u][2], o[u][3]}, (u4){o[u][4], o[u][5], o[u][6], o[u][7]});
        }
      } else {
        const int chl = 16 * n + c, seg = 4 * m + g;
        const f2 lam = lds_ld2(C_LAM + chl * 8), cc = lds_ld2(C_LAM + 512 + chl * 8);
        f2 st = {0.f, 0.f};
#pragma unroll
        for (int a = 0; a < 4; ++a) {
          const f4 mean4 = lds_ld4(C_MEAN + amr + 16 * a), rinv4 = lds_ld4(C_RINV + amr + 16 * a);
          f2 vr[2], vi[2], kx[2], ky[2], tr[2], ti[2];
#pragma unroll
          for (int i = 0; i < 2; ++i) {
            const f2 mn = {mean4[2 * i], mean4[2 * i + 1]}, ri = {rinv4[2 * i], rinv4[2 * i + 1]};
            vr[i] = __builtin_elementwise_fma(mn, (f2){ncs[0], ncs[0]}, (f2){acc[a][0][2 * i], acc[a][0][2 * i + 1]});
            vi[i] = __builtin_elementwise_fma(mn, (f2){ncs[1], ncs[1]}, (f2){acc[a][1][2 * i], acc[a][1][2 * i + 1]});
            kx[i] = ri * cc.x; ky[i] = ri * cc.y;
          }
          SCHED_FENCE();
#pragma unroll
          for (int i = 0; i < 2; ++i) { tr[i] = ky[i] * vi[i]; ti[i] = ky[i] * vr[i]; }
          SCHED_FENCE();
#pragma unroll
          for (int i = 0; i < 2; ++i) {
            const f2 br = __builtin_elementwise_fma(kx[i], vr[i], -tr[i]);
            const f2 bi = __builtin_elementwise_fma(-kx[i], vi[i], -ti[i]);
            acc[a][0][2 * i] = br.x; acc[a][0][2 * i + 1] = br.y; acc[a][1][2 * i] = bi.x; acc[a][1][2 * i + 1] = bi.y;
          }
          SCHED_FENCE();
        }
#pragma unroll
        for (int a = 0; a < 4; ++a)
#pragma unroll
          for (int i = 0; i < 4; ++i) st = cmul_add(lam, st, (f2){acc[a][0][i], acc[a][1][i]});
        lds_st2(C_AGG + (seg * 64 + chl) * 8, st);
        LDS_SYNC();
        STAMP(6);
        LDS_SYNC();
        STAMP(7);
        f2 h = lds_ld2(C_CIN + (seg * 64 + chl) * 8);
        u4 ore[2], oim[2];
#pragma unroll
        for (int a = 0; a < 4; ++a) {
          float hr[4], hi[4];
#pragma unroll
          for (int i = 0; i < 4; ++i) {
            h = cmul_add(lam, h, (f2){acc[a][0][i], acc[a][1][i]});
            hr[i] = h.x; hi[i] = h.y;
          }
          ore[a >> 1][2 * (a & 1)] = pack2(hr[0], hr[1]); ore[a >> 1][2 * (a & 1) + 1] = pack2(hr[2], hr[3]);
          oim[a >> 1][2 * (a & 1)] = pack2(hi[0], hi[1]); oim[a >> 1][2 * (a & 1) + 1] = pack2(hi[2], hi[3]);
        }
        store_tile(ht, j * 64 + 16 * n, c, g, ore[0], ore[1]);
        store_tile(ht, 512 + j * 64 + 16 * n, c, g, oim[0], oim[1]);
      }
    }
    STAMP(8);
    if (!SSM) {
      asm volatile("s_waitcnt vmcnt(0)" ::: "memory");
      BARRIER();
    }
    STAMP(9);

    const int kq = w & 3, th = w >> 2;
    unsigned adT[4], adN;
    {
      const int q = c >> 2, pp = c & 3, key = ((q >> 1) & 1) | ((g & 1) << 1);
#pragma unroll
      for (int u = 0; u < 4; ++u) adT[u] = (32 * kq + 8 * g + q) * 128 + ((((u ^ key) << 1) | (pp >> 1)) << 4) + 8 * (pp & 1);
      adN = (kq >> 1) * 8192 + c * 128 + (((4 * (kq & 1) + g) ^ (c & 7)) << 4);
    }
    const unsigned adH0 = 16384 + (th ? adT[2] : adT[0]), adH1 = 16384 + (th ? adT[3] : adT[1]);
    f4 ac2[4][2];
#pragma unroll
    for (int a = 0; a < 4; ++a) { ac2[a][0] = (f4){0.f, 0.f, 0.f, 0.f}; ac2[a][1] = (f4){0.f, 0.f, 0.f, 0.f}; }
    for (int s = 0; s < 19; ++s) {
      if (SSM && s == DRAIN_S) asm volatile("s_waitcnt vmcnt(0)" ::: "memory");
      LDS_SYNC();
      const unsigned slot = (unsigned)(s & 3) * 32768;
      if (s == 10 && kq == 3) continue;
      bf8 W[4];
      if (s >= 11) {
#pragma unroll
        for (int a = 0; a < 4; ++a) W[a] = lds_b128(slot + adN + a * 2048);
      } else {
#pragma unroll
        for (int a = 0; a < 4; ++a) W[a] = lds_tr(slot + adT[a]);
      }
      const bf8 H0 = lds_tr(slot + adH0), H1 = lds_tr(slot + adH1);
#pragma unroll
      for (int a = 0; a < 4; ++a) { ac2[a][0] = mfma(W[a], H0, ac2[a][0]); ac2[a][1] = mfma(W[a], H1, ac2[a][1]); }
    }
    LDS_SYNC();
#pragma unroll
    for (int a = 0; a < 4; ++a)
#pragma unroll
      for (int u = 0; u < 2; ++u) {
        const int t = 32 * th + 16 * u + c;
        lds_st4(kq * 16384 + t * 256 + (((4 * a + g) ^ (t & 7)) << 4), ac2[a][u]);
      }
  }
  u2 xr[2], dr;
  if (w < 8) {
    const unsigned ox = (unsigned)((8 * w + g) * D + d0 + 4 * c) * 2, od = (unsigned)(d0 + 4 * c) * 2;
    const char* xb = (const char*)(p.x + (size_t)tt * 64 * D);
    xr[0] = *(const u2*)(xb + ox); xr[1] = *(const u2*)(xb + 4 * D * 2 + ox);
    dr = *(const u2*)((const char*)p.dsk + od);
  }
  LDS_SYNC();
  if (w < 8) {
    const f4 dsk = {__uint_as_float(dr.x << 16), __uint_as_float(dr.x & 0xffff0000u), __uint_as_float(dr.y << 16), __uint_as_float(dr.y & 0xffff0000u)};
#pragma unroll
    for (int r = 0; r < 2; ++r) {
      const int t = 8 * w + 4 * r + g;
      const unsigned a = t * 256 + ((c ^ (t & 7)) << 4);
      f4 v = lds_ld4(a) + lds_ld4(a + 16384) + lds_ld4(a + 32768) + lds_ld4(a + 49152);
      const int tl = hf * 64 + t;
      const float mb = lds_ld(C_MEAN + tl * 4), ri = lds_ld(C_RINV + tl * 4);
      const float xv[4] = {__uint_as_float(xr[r].x << 16), __uint_as_float(xr[r].x & 0xffff0000u),
                           __uint_as_float(xr[r].y << 16), __uint_as_float(xr[r].y & 0xffff0000u)};
#pragma unroll
      for (int i = 0; i < 4; ++i) {
        const float xn = bfr(bfr(xv[i] - mb) * ri);
        v[i] = xv[i] + (v[i] + bfr(xn * dsk[i]));
      }
      *(f4*)(p.out + (size_t)(tt * 64 + t) * D + d0 + 4 * c) = v;
    }
  }
  STAMP(14);
  STAMP(15);
  STAMP_OUT(0);
}

__global__ __launch_bounds__(768, 3) void s5_fused(Params p) {
  const int b = blockIdx.x, q = b & 7, idx = b >> 3;
  const int rt = 4 * (q >> 1) + (idx >> 3), i8 = idx & 7, j = 4 * (q & 1) + (i8 & 3);
  if (i8 < 4) body<true>(p, rt, j, i8);
  else body<false>(p, rt, j, i8);
}

extern "C" void kernel_launch(void* const* d_in, const int* in_sizes, int n_in, void* d_out, int out_size,
                              void* d_ws, size_t ws_size, hipStream_t stream) {
  Params p;
  p.x = (const u16*)d_in[0]; p.lre = (const u16*)d_in[1]; p.lim = (const u16*)d_in[2];
  p.bre = (const u16*)d_in[3]; p.bim = (const u16*)d_in[4]; p.cre = (const u16*)d_in[5]; p.cim = (const u16*)d_in[6];
  p.ldt = (const u16*)d_in[7]; p.dsk = (const u16*)d_in[8]; p.w1 = (const u16*)d_in[10]; p.w2 = (const u16*)d_in[11];
  p.out = (float*)d_out; p.ws = (char*)d_ws;
  (void)hipFuncSetAttribute((const void*)s5_fused, hipFuncAttributeMaxDynamicSharedMemorySize, LDS_BYTES);
  s5_fused<<<256, 768, LDS_BYTES, stream>>>(p);
}
```

```cpp
#include <hip/hip_runtime.h>
#include <hip/hip_bf16.h>
#include <stdint.h>

typedef __attribute__((ext_vector_type(8))) __bf16 bf8;
typedef __attribute__((ext_vector_type(4))) __bf16 bf4;
typedef __attribute__((ext_vector_type(2))) __bf16 bf2;
typedef __attribute__((ext_vector_type(4))) float f4;
typedef __attribute__((ext_vector_type(2))) float f2;
typedef __attribute__((ext_vector_type(4))) unsigned u4;
typedef __attribute__((ext_vector_type(2))) unsigned u2;
typedef __attribute__((ext_vector_type(2))) short s2;
typedef unsigned long long u64;
typedef unsigned short u16;
#define LDS __attribute__((address_space(3)))
#define GLB __attribute__((address_space(1)))

constexpr int T = 2048, D = 512, P = 512, F = 1376;
constexpr int KH = 2432;
constexpr size_t WS_EPOCH = 0;
constexpr size_t WS_FLAG = 4096;
constexpr size_t WS_ROW = 8192;
constexpr size_t WS_REC = 65536;
constexpr size_t WS_STAMP = 128u << 20;
#define STAMP_DECL
#define STAMP(i)
#define STAMP_OUT(k)
constexpr size_t WS_H = 1 << 20;

__device__ __forceinline__ void glds_x4(const unsigned* vo, const char* sbase, unsigned lds_addr) {
  unsigned keep;
  asm volatile("s_mov_b32 %0, m0\n\ts_mov_b32 m0, %6\n\ts_nop 0\n\tglobal_load_lds_dwordx4 %1, %5\n\t"
               "global_load_lds_dwordx4 %2, %5 offset:1024\n\tglobal_load_lds_dwordx4 %3, %5 offset:2048\n\t"
               "global_load_lds_dwordx4 %4, %5 offset:3072\n\ts_mov_b32 m0, %0"
               : "=&s"(keep) : "v"(vo[0]), "v"(vo[1]), "v"(vo[2]), "v"(vo[3]), "s"(sbase - 4096), "s"(lds_addr));
}
__device__ __forceinline__ void glds_x4_dev(const unsigned* vo, const char* sbase, unsigned lds_addr) {
  unsigned keep;
  asm volatile("s_mov_b32 %0, m0\n\ts_mov_b32 m0, %6\n\ts_nop 0\n\tglobal_load_lds_dwordx4 %1, %5 sc1\n\t"
               "global_load_lds_dwordx4 %2, %5 offset:1024 sc1\n\tglobal_load_lds_dwordx4 %3, %5 offset:2048 sc1\n\t"
               "global_load_lds_dwordx4 %4, %5 offset:3072 sc1\n\ts_mov_b32 m0, %0"
               : "=&s"(keep) : "v"(vo[0]), "v"(vo[1]), "v"(vo[2]), "v"(vo[3]), "s"(sbase - 4096), "s"(lds_addr));
}
__device__ __forceinline__ void glds_x2(const unsigned* vo, const char* sbase, unsigned lds_addr) {
  unsigned keep;
  asm volatile("s_mov_b32 %0, m0\n\ts_mov_b32 m0, %4\n\ts_nop 0\n\tglobal_load_lds_dwordx4 %1, %3\n\t"
               "global_load_lds_dwordx4 %2, %3 offset:1024\n\ts_mov_b32 m0, %0"
               : "=&s"(keep) : "v"(vo[0]), "v"(vo[1]), "s"(sbase - 4096), "s"(lds_addr));
}
__device__ __forceinline__ void touch(unsigned voff, const void* sbase, unsigned lds_addr) {
  unsigned keep;
  asm volatile("s_mov_b32 %0, m0\n\ts_mov_b32 m0, %3\n\ts_nop 0\n\tglobal_load_lds_dword %1, %2\n\ts_mov_b32 m0, %0"
               : "=&s"(keep) : "v"(voff), "s"(sbase), "s"(lds_addr));
}
constexpr int NPF = 3;
template <int N> __device__ __forceinline__ void wait_vm() { asm volatile("s_waitcnt vmcnt(%0) lgkmcnt(0)" :: "n"(N) : "memory"); }
#define WAIT_VM(n) asm volatile("s_waitcnt vmcnt(" #n ") lgkmcnt(0)" ::: "memory")
#define BARRIER() do { __builtin_amdgcn_s_barrier(); asm volatile("" ::: "memory"); } while (0)
#define SCHED_FENCE() __builtin_amdgcn_sched_barrier(0)
#define LDS_SYNC() do { asm volatile("s_waitcnt lgkmcnt(0)" ::: "memory"); BARRIER(); } while (0)

__device__ __forceinline__ bf8 lds_b128(unsigned a) { return *(LDS const bf8*)(size_t)a; }
__device__ __forceinline__ bf8 lds_tr(unsigned a) {
  bf4 lo = __builtin_amdgcn_ds_read_tr16_b64_v4bf16((LDS bf4*)(size_t)a);
  bf4 hi = __builtin_amdgcn_ds_read_tr16_b64_v4bf16((LDS bf4*)(size_t)(a + 512));
  return __builtin_shufflevector(lo, hi, 0, 1, 2, 3, 4, 5, 6, 7);
}
__device__ __forceinline__ f4 mfma(bf8 a, bf8 b, f4 c) { return __builtin_amdgcn_mfma_f32_16x16x32_bf16(a, b, c, 0, 0, 0); }
__device__ __forceinline__ float bfr(float v) { return (float)(__bf16)v; }
__device__ __forceinline__ float bf2f(u16 v) { return __uint_as_float((unsigned)v << 16); }
__device__ __forceinline__ unsigned pack2(float a, float b) { return __builtin_bit_cast(unsigned, __builtin_convertvector((f2){a, b}, bf2)); }
__device__ __forceinline__ void lds_st(unsigned a, float v) { *(LDS float*)(size_t)a = v; }
__device__ __forceinline__ float lds_ld(unsigned a) { return *(LDS const float*)(size_t)a; }
__device__ __forceinline__ f4 lds_ld4(unsigned a) { return *(LDS const f4*)(size_t)a; }
__device__ __forceinline__ f2 lds_ld2(unsigned a) { return *(LDS const f2*)(size_t)a; }
__device__ __forceinline__ void lds_st2(unsigned a, f2 v) { *(LDS f2*)(size_t)a = v; }
__device__ __forceinline__ void lds_st4(unsigned a, f4 v) { *(LDS f4*)(size_t)a = v; }
__device__ __forceinline__ f2 cmul_add(f2 a, f2 b, f2 c) {
  return (f2){fmaf(-a.y, b.y, fmaf(a.x, b.x, c.x)), fmaf(a.y, b.x, fmaf(a.x, b.y, c.y))};
}
__device__ __forceinline__ f2 cmul(f2 a, f2 b) { return (f2){a.x * b.x - a.y * b.y, a.x * b.y + a.y * b.x}; }

struct Params {
  const u16* x; const u16* lre; const u16* lim; const u16* bre; const u16* bim; const u16* cre; const u16* cim;
  const u16* ldt; const u16* dsk; const u16* w1; const u16* w2; float* out; char* ws;
};

constexpr unsigned C_LAM = 131072;
constexpr unsigned C_MEAN = C_LAM + 1024;
constexpr unsigned C_RINV = C_MEAN + 512;
constexpr unsigned C_CS = C_RINV + 512;
constexpr unsigned C_TAG = C_CS + 1536;
constexpr unsigned C_AGG = C_TAG + 256;
constexpr unsigned C_CIN = C_AGG + 4096;
constexpr unsigned C_JUNK = C_CIN + 4096;
constexpr unsigned LDS_BYTES = C_JUNK + 256;
constexpr int SPIN_MAX = 400000;
constexpr int DRAIN_S = 2;

template <int CTRL> __device__ __forceinline__ float dpp_add(float v) {
  return v + __int_as_float(__builtin_amdgcn_update_dpp(0, __float_as_int(v), CTRL, 0xf, 0xf, true));
}
__device__ __forceinline__ float wave_sum(float v) {
  v = dpp_add<0x111>(v); v = dpp_add<0x112>(v); v = dpp_add<0x114>(v); v = dpp_add<0x118>(v);
  v += __int_as_float(__builtin_amdgcn_update_dpp(0, __float_as_int(v), 0x142, 0xa, 0xf, true));
  v += __int_as_float(__builtin_amdgcn_update_dpp(0, __float_as_int(v), 0x143, 0xc, 0xf, true));
  return __int_as_float(__builtin_amdgcn_readlane(__float_as_int(v), 63));
}
__device__ __forceinline__ void st_gran(char* base, unsigned idx, unsigned tag, unsigned val) {
  __hip_atomic_store((GLB u64*)base + idx, ((u64)tag << 32) | val, __ATOMIC_RELAXED, __HIP_MEMORY_SCOPE_AGENT);
}
__device__ __forceinline__ u64 ld_gran(char* base, unsigned idx) {
  return __hip_atomic_load((GLB u64*)base + idx, __ATOMIC_RELAXED, __HIP_MEMORY_SCOPE_AGENT);
}
__device__ __forceinline__ unsigned wait_gran(char* base, unsigned idx, unsigned tag, u64 v) {
  if (!__all((unsigned)(v >> 32) == tag)) {
    int spin = 0;
    do {
      __builtin_amdgcn_s_sleep(1);
      v = ld_gran(base, idx);
    } while (!__all((unsigned)(v >> 32) == tag) && ++spin < SPIN_MAX);
  }
  return (unsigned)v;
}
__device__ __forceinline__ void st16_dev(char* base, unsigned off, u4 v) {
  asm volatile("global_store_dwordx4 %0, %1, %2 sc1\n\ts_nop 1" :: "v"(off), "v"(v), "s"(base) : "memory");
}
__device__ __forceinline__ unsigned ror8(unsigned v) { return (unsigned)__builtin_amdgcn_update_dpp(0, (int)v, 0x128, 0xf, 0xf, true); }
__device__ __forceinline__ void store_tile(char* ht, unsigned k0, int c, int g, u4 lo, u4 hi) {
  const bool low = c < 8;
  u4 a, b;
#pragma unroll
  for (int i = 0; i < 4; ++i) {
    const unsigned wv = ror8(low ? hi[i] : lo[i]);
    a[i] = low ? lo[i] : wv; b[i] = low ? wv : hi[i];
  }
  const unsigned off = (k0 + (c & 7)) * 128 + (2 * g + (c >> 3)) * 16;
  st16_dev(ht, off, a);
  st16_dev(ht, off + 1024, b);
}
__device__ __forceinline__ void unpack8(u4 v, float* f) {
#pragma unroll
  for (int i = 0; i < 4; ++i) { f[2 * i] = __uint_as_float(v[i] << 16); f[2 * i + 1] = __uint_as_float(v[i] & 0xffff0000u); }
}

template <bool SSM>
__device__ __forceinline__ void body(const Params& p, int rt, int j, int i8) {
  constexpr int NT = SSM ? 2 : 3;
  constexpr int NSLOT = SSM ? 4 : 3;
  constexpr unsigned SLOT = SSM ? 32768 : 40960;
  const int tid = threadIdx.x, lane = tid & 63;
  const int w = __builtin_amdgcn_readfirstlane(tid >> 6);
  const int c = lane & 15, g = lane >> 4;
  const int t0 = rt * 128;
  const int jj = SSM ? j : j + 8;
  const int hf = j >> 2, r4 = rt & 3;
  const int tt = 2 * rt + hf, d0 = i8 * 64;
  STAMP_DECL; STAMP(0);
  int f_lo = 0, f_hi = 0, f_ld = 0;
  if (!SSM) { f_lo = 16 * ((86 * j) >> 3); f_hi = 16 * ((86 * (j + 1)) >> 3); f_ld = f_lo < F - 192 ? f_lo : F - 192; }

  if (w >= 8) {
    const int L = w - 8;
    unsigned r_lre = 0, r_lim = 0, r_ldt = 0;
    if (SSM && L == 0) {
      const unsigned o = (unsigned)(j * 64 + lane) * 2;
      asm volatile("global_load_ushort %0, %3, %4\n\tglobal_load_ushort %1, %3, %5\n\tglobal_load_ushort %2, %3, %6"
                   : "=&v"(r_lre), "=&v"(r_lim), "=&v"(r_ldt) : "v"(o), "s"(p.lre), "s"(p.lim), "s"(p.ldt));
    }
    unsigned voA[4], voB[6];
#pragma unroll
    for (int u = 0; u < 4; ++u) {
      const int row = 32 * L + 8 * u + (lane >> 3), key = (row & 3) | ((row >> 2) & 4);
      voA[u] = (unsigned)(row * D + (((lane & 7) ^ key) << 3)) * 2 + 4096 - 1024 * u;
    }
    const char* baseA = (const char*)(p.x + (size_t)t0 * D);
    const char* baseB;
    if (SSM) {
#pragma unroll
      for (int u = 0; u < 4; ++u) {
        const int row = (32 * L + 8 * u + (lane >> 3)) & 63;
        voB[u] = (unsigned)(row * D + (((lane & 7) ^ (row & 7)) << 3)) * 2 + 4096 - 1024 * u;
      }
      baseB = (const char*)((L < 2 ? p.bre : p.bim) + (size_t)j * 64 * D);
    } else {
#pragma unroll
      for (int u = 0; u < 6; ++u) {
        const int i = 6 * L + u, k = 8 * (i & 7) + (lane >> 3), key = ((k >> 1) & 1) | ((k >> 2) & 2);
        voB[u] = (unsigned)(k * F + 64 * (i >> 3) + (((lane & 7) ^ (key << 1)) << 3)) * 2 + 4096 - 1024 * (u & 3);
      }
      baseB = (const char*)(p.w1 + f_ld);
    }
    auto stage = [&](int s) {
      const int ks = s;
      const unsigned slot = (unsigned)(s % NSLOT) * SLOT;
      glds_x4(voA, baseA + ks * 128, slot + L * 4096);
      if (SSM) {
        glds_x4(voB, baseB + ks * 128, slot + 16384 + L * 4096);
      } else {
        const char* b = baseB + (size_t)ks * 64 * F * 2;
        glds_x4(voB, b, slot + 16384 + L * 6144);
        glds_x2(voB + 4, b, slot + 16384 + L * 6144 + 4096);
      }
    };
#pragma unroll
    for (int s = 0; s < NSLOT - 1; ++s) stage(s);
    char* const gbase = p.ws + WS_ROW;
    const unsigned gi0 = t0 + 64 * (L & 1) + lane;
    u64 g0 = 0;
    unsigned tag = 0;
#pragma unroll
    for (int s = 0; s < 8; ++s) {
      if (SSM) { if (s < 5) WAIT_VM(16); else if (s == 5) wait_vm<17 + NPF>(); else if (s == 6) wait_vm<9 + NPF>(); else wait_vm<NPF>(); }
      else     { if (s < 6) WAIT_VM(10); else if (s == 6) wait_vm<10 + NPF>(); else wait_vm<NPF>(); }
      BARRIER();
      if (s == 4) asm volatile("global_load_dwordx2 %0, %1, %2 sc1" : "=&v"(g0) : "v"(gi0 * 8), "s"(gbase));
      if (s + NSLOT - 1 < 8) stage(s + NSLOT - 1);
      if (s + NSLOT - 1 == 7) {
        const unsigned sh = r4 * 8 + i8;
        touch((sh * 128 + (L & 1) * 64 + lane) * 128, L < 2 ? p.cre : p.cim, C_JUNK);
        touch((sh * 344 + L * 86 + lane) * 128, p.w2, C_JUNK);
        touch((sh * 344 + L * 86 + 64 + (lane < 22 ? lane : 21)) * 128, p.w2, C_JUNK);
      }
      if (s == 0) {
        tag = *(LDS const unsigned*)(size_t)C_TAG;
        if (SSM && L == 0) {
          asm volatile("" : "+v"(r_lre), "+v"(r_lim), "+v"(r_ldt));
          const float lr = bf2f((u16)r_lre), li = bf2f((u16)r_lim);
          const float dt = bfr(expf(bf2f((u16)r_ldt)));
          const float e = expf(dt * lr);
          float sn, cn; sincosf(dt * li, &sn, &cn);
          const f2 lam = {e * cn, e * sn};
          const float nr = lam.x - 1.0f, ni = lam.y, den = lr * lr + li * li;
          const float inv = 2.0f / den;
          float n0 = fmaf(nr, lr, ni * li), n1 = fmaf(ni, lr, -nr * li);
          asm volatile("" : "+v"(n0), "+v"(n1));
          lds_st2(C_LAM + lane * 8, (f2){lam.x, -lam.y});
          lds_st2(C_LAM + 512 + lane * 8, (f2){n0 * inv, n1 * inv});
        }
      }
      if (s == 7) {
        asm volatile("" : "+v"(g0));
        if (L < 2) {
          const unsigned v0 = wait_gran(gbase, gi0, tag, g0);
          lds_st(C_MEAN + (64 * L + lane) * 4, __uint_as_float(v0 & 0xffff0000u));
          lds_st(C_RINV + (64 * L + lane) * 4, 1.0f / __uint_as_float(v0 << 16));
        }
      }
    }
    LDS_SYNC();

    unsigned voH[4], voN[4], voK[4];
#pragma unroll
    for (int u = 0; u < 4; ++u) {
      const int i = 4 * L + u, r8 = lane >> 3, pos = lane & 7;
      const int k = 8 * i + r8, key = ((k >> 1) & 1) | ((k >> 2) & 2);
      const int chunk = pos ^ (key << 1);
      voH[u] = (unsigned)(k * 128 + chunk * 16) + 4096 - 1024 * u;
      voK[u] = (unsigned)(k * (D * 2) + chunk * 16) + 4096 - 1024 * u;
      const int row = 8 * (i & 7) + r8;
      voN[u] = (unsigned)(row * (P * 2) + (i >> 3) * 128 + ((pos ^ (row & 7)) << 4)) + 4096 - 1024 * u;
    }
    const char* hbase = p.ws + WS_H + (size_t)tt * KH * 128;
    auto stageW = [&](int s) {
      const unsigned slot = (unsigned)(s & 3) * 32768;
      if (s >= 11) {
        const int st = s - 11;
        glds_x4(voN, (const char*)((st < 4 ? p.cre : p.cim) + (size_t)d0 * P + (st & 3) * 128), slot + L * 4096);
      } else {
        const char* wb = (const char*)(p.w2 + (size_t)s * 128 * D + d0);
        if (s == 10 && L == 3) wb -= 32 * D * 2;
        glds_x4(voK, wb, slot + L * 4096);
      }
    };
    auto stageH = [&](int s) {
      const int kb = s >= 11 ? s - 11 : s + 8;
      glds_x4_dev(voH, hbase + (size_t)kb * 16384, (unsigned)(s & 3) * 32768 + 16384 + L * 4096);
    };
    if (!(SSM && L == 0)) { stageW(0); stageW(1); stageW(2); }
    if (SSM) {
      f2 l16 = {0.f, 0.f}, l128 = {0.f, 0.f};
      if (L == 0) {
        l16 = lds_ld2(C_LAM + lane * 8);
#pragma unroll
        for (int i = 0; i < 4; ++i) l16 = cmul(l16, l16);
        l128 = l16;
#pragma unroll
        for (int i = 0; i < 3; ++i) l128 = cmul(l128, l128);
      }
      LDS_SYNC();
      if (L == 0) {
        f2 ag[8];
#pragma unroll
        for (int i = 0; i < 8; ++i) ag[i] = lds_ld2(C_AGG + (i * 64 + lane) * 8);
        f2 tot = ag[0];
#pragma unroll
        for (int i = 1; i < 8; ++i) tot = cmul_add(l16, tot, ag[i]);
        char* rec = p.ws + WS_REC + (size_t)j * 1024;
        if (rt < 15) {
          st_gran(rec, rt * 1024 + lane, tag, __float_as_uint(tot.x));
          st_gran(rec, rt * 1024 + 64 + lane, tag, __float_as_uint(tot.y));
        }
        f2 carry = {0.f, 0.f};
        if (rt > 0) {
          u64 vr[15], vi[15];
          for (int spin = 0; spin < SPIN_MAX; ++spin) {
            bool ok = true;
#pragma unroll
            for (int i = 0; i < 15; ++i) {
              const int ii = i < rt ? i : 0;
              vr[i] = ld_gran(rec, ii * 1024 + lane);
              vi[i] = ld_gran(rec, ii * 1024 + 64 + lane);
            }
#pragma unroll
            for (int i = 0; i < 15; ++i) ok = ok && (unsigned)(vr[i] >> 32) == tag && (unsigned)(vi[i] >> 32) == tag;
            if (__all(ok)) break;
            __builtin_amdgcn_s_sleep(1);
          }
#pragma unroll
          for (int i = 0; i < 15; ++i)
            if (i < rt) carry = cmul_add(l128, carry, (f2){__uint_as_float((unsigned)vr[i]), __uint_as_float((unsigned)vi[i])});
        }
#pragma unroll
        for (int i = 0; i < 8; ++i) {
          lds_st2(C_CIN + (i * 64 + lane) * 8, carry);
          carry = cmul_add(l16, carry, ag[i]);
        }
      }
      LDS_SYNC();
      if (L == 0) { stageW(0); stageW(1); stageW(2); }
    }
    char* const flags = p.ws + WS_FLAG;
    if (!SSM) {
      BARRIER();
      if (L == 0 && lane == 0) st_gran(flags, rt * 16 + jj, tag, 0);
    }
    {
      const unsigned fi = rt * 16 + 8 + (lane & 7);
      u64 v;
      asm volatile("global_load_dwordx2 %0, %1, %2 sc1\n\ts_waitcnt vmcnt(0)" : "=&v"(v) : "v"(fi * 8), "s"(flags) : "memory");
      wait_gran(flags, fi, tag, v);
      asm volatile("s_waitcnt vmcnt(0)" ::: "memory");
    }
    stageH(0); stageH(1); stageH(2);
    u64 fl;
#pragma unroll
    for (int s = 0; s < 19; ++s) {
      if (s == 0) WAIT_VM(8); else if (s == 1) WAIT_VM(12);
      else if (s == 6 || s == 7) WAIT_VM(17);
      else if (s < 17) WAIT_VM(16); else if (s == 17) WAIT_VM(8); else WAIT_VM(0);
      BARRIER();
      if (s == 8) {
        asm volatile("" : "+v"(fl));
        wait_gran(flags, rt * 16 + (lane & 7), tag, fl);
      }
      if (s + 3 < 19) { stageW(s + 3); stageH(s + 3); }
      if (SSM && s == DRAIN_S && L == 0 && lane == 0) st_gran(flags, rt * 16 + jj, tag, 0);
      if (s == 5) asm volatile("global_load_dwordx2 %0, %1, %2 sc1" : "=&v"(fl) : "v"((rt * 16 + (lane & 7)) * 8), "s"(flags));
    }
    if (L == 1 && rt == 0 && jj == 0) {
#pragma unroll
      for (int i = 0; i < 4; ++i) wait_gran(p.ws + WS_ROW, (i * 64 + lane) * 8, tag, ld_gran(p.ws + WS_ROW, (i * 64 + lane) * 8));
      if (lane == 0) __hip_atomic_store((GLB unsigned*)(p.ws + WS_EPOCH), tag, __ATOMIC_RELAXED, __HIP_MEMORY_SCOPE_AGENT);
    }
    LDS_SYNC();
  } else {
    const int m = w >> 2, n = w & 3;
    unsigned rowv;
    {
      u4 xv;
      unsigned ep;
      const unsigned xo = (unsigned)((8 * jj + w) * D + lane * 8) * 2, z = 0;
      asm volatile("global_load_dwordx4 %0, %2, %4\n\tglobal_load_dword %1, %3, %5 sc1\n\ts_waitcnt vmcnt(0)"
                   : "=&v"(xv), "=&v"(ep) : "v"(xo), "v"(z), "s"(p.x + (size_t)t0 * D), "s"(p.ws + WS_EPOCH) : "memory");
      if (w == 0 && lane == 0) *(LDS unsigned*)(size_t)C_TAG = ep + 1u;
      float f[8];
      unpack8(xv, f);
      float sm = 0.f;
#pragma unroll
      for (int i = 0; i < 8; ++i) sm += f[i];
      const float mean = wave_sum(sm) * (1.0f / D);
      float sq = 0.f;
#pragma unroll
      for (int i = 0; i < 8; ++i) { const float dd = f[i] - mean; sq = fmaf(dd, dd, sq); }
      const float var = wave_sum(sq) * (1.0f / D);
      const float mb = bfr(mean), den = bfr(sqrtf(bfr(bfr(var) + 1e-5f)));
      rowv = (__float_as_uint(mb) & 0xffff0000u) | (__float_as_uint(den) >> 16);
      unsigned adA[2], adB[3];
      {
        const int row = 64 * m + 16 * (c >> 2) + (c & 3);
        adA[0] = row * 128 + ((g ^ (c & 7)) << 4);
        adA[1] = adA[0] ^ 64;
        if (SSM) {
          adB[0] = 16384 + (16 * n + c) * 128 + ((g ^ (c & 7)) << 4);
          adB[1] = adB[0] ^ 64;
        } else {
          const int q = c >> 2, pp = c & 3, key = ((q >> 1) & 1) | ((g & 1) << 1);
#pragma unroll
          for (int u = 0; u < 3; ++u) {
            const int ct = 3 * n + u;
            adB[u] = 16384 + (ct >> 2) * 8192 + (8 * g + q) * 128 + (((((ct & 3) ^ key) << 1) | (pp >> 1)) << 4) + 8 * (pp & 1);
          }
        }
      }
      f4 acc[4][NT], asum[NT];
      const __bf16 one = (__bf16)1.0f;
      const bf8 ones = {one, one, one, one, one, one, one, one};
#pragma unroll
      for (int u = 0; u < NT; ++u) asum[u] = (f4){0.f, 0.f, 0.f, 0.f};
#pragma unroll
      for (int a = 0; a < 4; ++a)
#pragma unroll
        for (int u = 0; u < NT; ++u) acc[a][u] = (f4){0.f, 0.f, 0.f, 0.f};
      bf8 A1[4], B1[NT];
      auto frags = [&](unsigned slot, int ks, bf8* A, bf8* B) {
#pragma unroll
        for (int a = 0; a < 4; ++a) A[a] = lds_b128(slot + adA[ks] + 512 * a);
        if (SSM) {
          B[0] = lds_b128(slot + adB[ks]);
          B[1] = lds_b128(slot + adB[ks] + 8192);
        } else {
#pragma unroll
          for (int u = 0; u < 3; ++u) B[u] = lds_tr(slot + adB[u] + ks * 4096);
        }
      };
      auto mm = [&](const bf8* A, const bf8* B) {
#pragma unroll
        for (int a = 0; a < 4; ++a)
#pragma unroll
          for (int u = 0; u < NT; ++u) acc[a][u] = mfma(A[a], B[u], acc[a][u]);
      };
#pragma unroll
      for (int s = 0; s < 8; ++s) {
        LDS_SYNC();
        STAMP(1 + (s >> 1));
        if (s == 0) {
          const unsigned tag = *(LDS const unsigned*)(size_t)C_TAG;
          if (lane == 0) st_gran(p.ws + WS_ROW, t0 + 8 * jj + w, tag, rowv);
        }
        const unsigned slot = (unsigned)(s % NSLOT) * SLOT;
        bf8 A0[4], B0[NT];
        frags(slot, 0, A0, B0);
        SCHED_FENCE();
        if (s > 0) mm(A1, B1);
        SCHED_FENCE();
#pragma unroll
        for (int u = 0; u < NT; ++u)
          if (s > 0) asum[u] = mfma(ones, m ? B1[u] : B0[u], asum[u]);
          else if (m == 0) asum[u] = mfma(ones, B0[u], asum[u]);
        SCHED_FENCE();
        frags(slot, 1, A1, B1);
        SCHED_FENCE();
        mm(A0, B0);
        SCHED_FENCE();
      }
      mm(A1, B1);
      if (m) {
#pragma unroll
        for (int u = 0; u < NT; ++u) asum[u] = mfma(ones, B1[u], asum[u]);
      }
      if (g == 0) {
#pragma unroll
        for (int u = 0; u < NT; ++u) lds_st(C_CS + (m * 192 + (NT * n + u) * 16 + c) * 4, asum[u][0]);
      }
      LDS_SYNC();
      STAMP(5);
      float ncs[NT];
#pragma unroll
      for (int u = 0; u < NT; ++u)
        ncs[u] = -(lds_ld(C_CS + ((NT * n + u) * 16 + c) * 4) + lds_ld(C_CS + (192 + (NT * n + u) * 16 + c) * 4));
      char* ht = p.ws + WS_H + (size_t)(2 * rt + m) * KH * 128;
      const unsigned amr = (64 * m + 16 * g) * 4;
      if (!SSM) {
        unsigned o[3][8];
#pragma unroll
        for (int a = 0; a < 4; ++a) {
          const f4 mean4 = lds_ld4(C_MEAN + amr + 16 * a), rinv4 = lds_ld4(C_RINV + amr + 16 * a);
          f2 v[6];
#pragma unroll
          for (int q = 0; q < 6; ++q) {
            const int u = q >> 1, i = q & 1;
            v[q] = __builtin_elementwise_fma((f2){mean4[2 * i], mean4[2 * i + 1]}, (f2){ncs[u], ncs[u]}, (f2){acc[a][u][2 * i], acc[a][u][2 * i + 1]});
          }
          SCHED_FENCE();
#pragma unroll
          for (int q = 0; q < 6; ++q) v[q] *= (f2){rinv4[2 * (q & 1)], rinv4[2 * (q & 1) + 1]};
          SCHED_FENCE();
          unsigned pk[6];
#pragma unroll
          for (int q = 0; q < 6; ++q) pk[q] = pack2(v[q].x, v[q].y);
          SCHED_FENCE();
#pragma unroll
          for (int q = 0; q < 6; ++q)
            o[q >> 1][2 * a + (q & 1)] = __builtin_bit_cast(unsigned, __builtin_elementwise_max(__builtin_bit_cast(s2, pk[q]), (s2){0, 0}));
          SCHED_FENCE();
        }
#pragma unroll
        for (int u = 0; u < 3; ++u) {
          const int f0 = f_ld + 16 * (3 * n + u);
          if (f0 >= f_lo && f0 < f_hi)
            store_tile(ht, 1024 + f0, c, g, (u4){o[u][0], o[u][1], o[u][2], o[u][3]}, (u4){o[u][4], o[u][5], o[u][6], o[u][7]});
        }
      } else {
        const int chl = 16 * n + c, seg = 4 * m + g;
        const f2 lam = lds_ld2(C_LAM + chl * 8), cc = lds_ld2(C_LAM + 512 + chl * 8);
        f2 st = {0.f, 0.f};
#pragma unroll
        for (int a = 0; a < 4; ++a) {
          const f4 mean4 = lds_ld4(C_MEAN + amr + 16 * a), rinv4 = lds_ld4(C_RINV + amr + 16 * a);
          f2 vr[2], vi[2], kx[2], ky[2], tr[2], ti[2];
#pragma unroll
          for (int i = 0; i < 2; ++i) {
            const f2 mn = {mean4[2 * i], mean4[2 * i + 1]}, ri = {rinv4[2 * i], rinv4[2 * i + 1]};
            vr[i] = __builtin_elementwise_fma(mn, (f2){ncs[0], ncs[0]}, (f2){acc[a][0][2 * i], acc[a][0][2 * i + 1]});
            vi[i] = __builtin_elementwise_fma(mn, (f2){ncs[1], ncs[1]}, (f2){acc[a][1][2 * i], acc[a][1][2 * i + 1]});
            kx[i] = ri * cc.x; ky[i] = ri * cc.y;
          }
          SCHED_FENCE();
#pragma unroll
          for (int i = 0; i < 2; ++i) { tr[i] = ky[i] * vi[i]; ti[i] = ky[i] * vr[i]; }
          SCHED_FENCE();
#pragma unroll
          for (int i = 0; i < 2; ++i) {
            const f2 br = __builtin_elementwise_fma(kx[i], vr[i], -tr[i]);
            const f2 bi = __builtin_elementwise_fma(-kx[i], vi[i], -ti[i]);
            acc[a][0][2 * i] = br.x; acc[a][0][2 * i + 1] = br.y; acc[a][1][2 * i] = bi.x; acc[a][1][2 * i + 1] = bi.y;
          }
          SCHED_FENCE();
        }
#pragma unroll
        for (int a = 0; a < 4; ++a)
#pragma unroll
          for (int i = 0; i < 4; ++i) st = cmul_add(lam, st, (f2){acc[a][0][i], acc[a][1][i]});
        lds_st2(C_AGG + (seg * 64 + chl) * 8, st);
        LDS_SYNC();
        STAMP(6);
        LDS_SYNC();
        STAMP(7);
        f2 h = lds_ld2(C_CIN + (seg * 64 + chl) * 8);
        u4 ore[2], oim[2];
#pragma unroll
        for (int a = 0; a < 4; ++a) {
          float hr[4], hi[4];
#pragma unroll
          for (int i = 0; i < 4; ++i) {
            h = cmul_add(lam, h, (f2){acc[a][0][i], acc[a][1][i]});
            hr[i] = h.x; hi[i] = h.y;
          }
          ore[a >> 1][2 * (a & 1)] = pack2(hr[0], hr[1]); ore[a >> 1][2 * (a & 1) + 1] = pack2(hr[2], hr[3]);
          oim[a >> 1][2 * (a & 1)] = pack2(hi[0], hi[1]); oim[a >> 1][2 * (a & 1) + 1] = pack2(hi[2], hi[3]);
        }
        store_tile(ht, j * 64 + 16 * n, c, g, ore[0], ore[1]);
        store_tile(ht, 512 + j * 64 + 16 * n, c, g, oim[0], oim[1]);
      }
    }
    STAMP(8);
    if (!SSM) {
      asm volatile("s_waitcnt vmcnt(0)" ::: "memory");
      BARRIER();
    }
    STAMP(9);

    const int kq = w & 3, th = w >> 2;
    unsigned adT[4], adN;
    {
      const int q = c >> 2, pp = c & 3, key = ((q >> 1) & 1) | ((g & 1) << 1);
#pragma unroll
      for (int u = 0; u < 4; ++u) adT[u] = (32 * kq + 8 * g + q) * 128 + ((((u ^ key) << 1) | (pp >> 1)) << 4) + 8 * (pp & 1);
      adN = (kq >> 1) * 8192 + c * 128 + (((4 * (kq & 1) + g) ^ (c & 7)) << 4);
    }
    const unsigned adH0 = 16384 + (th ? adT[2] : adT[0]), adH1 = 16384 + (th ? adT[3] : adT[1]);
    f4 ac2[4][2];
#pragma unroll
    for (int a = 0; a < 4; ++a) { ac2[a][0] = (f4){0.f, 0.f, 0.f, 0.f}; ac2[a][1] = (f4){0.f, 0.f, 0.f, 0.f}; }
    for (int s = 0; s < 19; ++s) {
      if (SSM && s == DRAIN_S) asm volatile("s_waitcnt vmcnt(0)" ::: "memory");
      LDS_SYNC();
      const unsigned slot = (unsigned)(s & 3) * 32768;
      if (s == 10 && kq == 3) continue;
      bf8 W[4];
      if (s >= 11) {
#pragma unroll
        for (int a = 0; a < 4; ++a) W[a] = lds_b128(slot + adN + a * 2048);
      } else {
#pragma unroll
        for (int a = 0; a < 4; ++a) W[a] = lds_tr(slot + adT[a]);
      }
      const bf8 H0 = lds_tr(slot + adH0), H1 = lds_tr(slot + adH1);
#pragma unroll
      for (int a = 0; a < 4; ++a) { ac2[a][0] = mfma(W[a], H0, ac2[a][0]); ac2[a][1] = mfma(W[a], H1, ac2[a][1]); }
    }
    LDS_SYNC();
#pragma unroll
    for (int a = 0; a < 4; ++a)
#pragma unroll
      for (int u = 0; u < 2; ++u) {
        const int t = 32 * th + 16 * u + c;
        lds_st4(kq * 16384 + t * 256 + (((4 * a + g) ^ (t & 7)) << 4), ac2[a][u]);
      }
  }
  u2 xr[2], dr;
  if (w < 8) {
    const unsigned ox = (unsigned)((8 * w + g) * D + d0 + 4 * c) * 2, od = (unsigned)(d0 + 4 * c) * 2;
    const char* xb = (const char*)(p.x + (size_t)tt * 64 * D);
    xr[0] = *(const u2*)(xb + ox); xr[1] = *(const u2*)(xb + 4 * D * 2 + ox);
    dr = *(const u2*)((const char*)p.dsk + od);
  }
  LDS_SYNC();
  if (w < 8) {
    const f4 dsk = {__uint_as_float(dr.x << 16), __uint_as_float(dr.x & 0xffff0000u), __uint_as_float(dr.y << 16), __uint_as_float(dr.y & 0xffff0000u)};
#pragma unroll
    for (int r = 0; r < 2; ++r) {
      const int t = 8 * w + 4 * r + g;
      const unsigned a = t * 256 + ((c ^ (t & 7)) << 4);
      f4 v = lds_ld4(a) + lds_ld4(a + 16384) + lds_ld4(a + 32768) + lds_ld4(a + 49152);
      const int tl = hf * 64 + t;
      const float mb = lds_ld(C_MEAN + tl * 4), ri = lds_ld(C_RINV + tl * 4);
      const float xv[4] = {__uint_as_float(xr[r].x << 16), __uint_as_float(xr[r].x & 0xffff0000u),
                           __uint_as_float(xr[r].y << 16), __uint_as_float(xr[r].y & 0xffff0000u)};
#pragma unroll
      for (int i = 0; i < 4; ++i) {
        const float xn = bfr(bfr(xv[i] - mb) * ri);
        v[i] = xv[i] + (v[i] + bfr(xn * dsk[i]));
      }
      st16_dev((char*)(p.out + (size_t)tt * 64 * D + d0), (unsigned)(t * D + 4 * c) * 4, __builtin_bit_cast(u4, v));
    }
  }
  STAMP(14);
  STAMP(15);
  STAMP_OUT(0);
}

__global__ __launch_bounds__(768, 3) void s5_fused(Params p) {
  const int b = blockIdx.x, q = b & 7, idx = b >> 3;
  const int rt = 4 * (q >> 1) + (idx >> 3), i8 = idx & 7, j = 4 * (q & 1) + (i8 & 3);
  if (i8 < 4) body<true>(p, rt, j, i8);
  else body<false>(p, rt, j, i8);
}

extern "C" void kernel_launch(void* const* d_in, const int* in_sizes, int n_in, void* d_out, int out_size,
                              void* d_ws, size_t ws_size, hipStream_t stream) {
  Params p;
  p.x = (const u16*)d_in[0]; p.lre = (const u16*)d_in[1]; p.lim = (const u16*)d_in[2];
  p.bre = (const u16*)d_in[3]; p.bim = (const u16*)d_in[4]; p.cre = (const u16*)d_in[5]; p.cim = (const u16*)d_in[6];
  p.ldt = (const u16*)d_in[7]; p.dsk = (const u16*)d_in[8]; p.w1 = (const u16*)d_in[10]; p.w2 = (const u16*)d_in[11];
  p.out = (float*)d_out; p.ws = (char*)d_ws;
  (void)hipFuncSetAttribute((const void*)s5_fused, hipFuncAttributeMaxDynamicSharedMemorySize, LDS_BYTES);
  s5_fused<<<256, 768, LDS_BYTES, stream>>>(p);
}
```

```cpp
#include <hip/hip_runtime.h>
#include <hip/hip_bf16.h>
#include <stdint.h>

typedef __attribute__((ext_vector_type(8))) __bf16 bf8;
typedef __attribute__((ext_vector_type(4))) __bf16 bf4;
typedef __attribute__((ext_vector_type(2))) __bf16 bf2;
typedef __attribute__((ext_vector_type(4))) float f4;
typedef __attribute__((ext_vector_type(2))) float f2;
typedef __attribute__((ext_vector_type(4))) unsigned u4;
typedef __attribute__((ext_vector_type(2))) unsigned u2;
typedef __attribute__((ext_vector_type(2))) short s2;
typedef unsigned long long u64;
typedef unsigned short u16;
#define LDS __attribute__((address_space(3)))
#define GLB __attribute__((address_space(1)))

constexpr int T = 2048, D = 512, P = 512, F = 1376;
constexpr int KH = 2432;
constexpr size_t WS_EPOCH = 0;
constexpr size_t WS_FLAG = 4096;
constexpr size_t WS_ROW = 8192;
constexpr size_t WS_REC = 65536;
constexpr size_t WS_H = 1 << 20;

__device__ __forceinline__ void glds_x4(const unsigned* vo, const char* sbase, unsigned lds_addr) {
  unsigned keep;
  asm volatile("s_mov_b32 %0, m0\n\ts_mov_b32 m0, %6\n\ts_nop 0\n\tglobal_load_lds_dwordx4 %1, %5\n\t"
               "global_load_lds_dwordx4 %2, %5 offset:1024\n\tglobal_load_lds_dwordx4 %3, %5 offset:2048\n\t"
               "global_load_lds_dwordx4 %4, %5 offset:3072\n\ts_mov_b32 m0, %0"
               : "=&s"(keep) : "v"(vo[0]), "v"(vo[1]), "v"(vo[2]), "v"(vo[3]), "s"(sbase - 4096), "s"(lds_addr));
}
__device__ __forceinline__ void glds_x4_dev(const unsigned* vo, const char* sbase, unsigned lds_addr) {
  unsigned keep;
  asm volatile("s_mov_b32 %0, m0\n\ts_mov_b32 m0, %6\n\ts_nop 0\n\tglobal_load_lds_dwordx4 %1, %5 sc1\n\t"
               "global_load_lds_dwordx4 %2, %5 offset:1024 sc1\n\tglobal_load_lds_dwordx4 %3, %5 offset:2048 sc1\n\t"
               "global_load_lds_dwordx4 %4, %5 offset:3072 sc1\n\ts_mov_b32 m0, %0"
               : "=&s"(keep) : "v"(vo[0]), "v"(vo[1]), "v"(vo[2]), "v"(vo[3]), "s"(sbase - 4096), "s"(lds_addr));
}
__device__ __forceinline__ void glds_x2(const unsigned* vo, const char* sbase, unsigned lds_addr) {
  unsigned keep;
  asm volatile("s_mov_b32 %0, m0\n\ts_mov_b32 m0, %4\n\ts_nop 0\n\tglobal_load_lds_dwordx4 %1, %3\n\t"
               "global_load_lds_dwordx4 %2, %3 offset:1024\n\ts_mov_b32 m0, %0"
               : "=&s"(keep) : "v"(vo[0]), "v"(vo[1]), "s"(sbase - 4096), "s"(lds_addr));
}
__device__ __forceinline__ void touch(unsigned voff, const void* sbase, unsigned lds_addr) {
  unsigned keep;
  asm volatile("s_mov_b32 %0, m0\n\ts_mov_b32 m0, %3\n\ts_nop 0\n\tglobal_load_lds_dword %1, %2\n\ts_mov_b32 m0, %0"
               : "=&s"(keep) : "v"(voff), "s"(sbase), "s"(lds_addr));
}
constexpr int NPF = 3;
template <int N> __device__ __forceinline__ void wait_vm() { asm volatile("s_waitcnt vmcnt(%0) lgkmcnt(0)" :: "n"(N) : "memory"); }
#define WAIT_VM(n) asm volatile("s_waitcnt vmcnt(" #n ") lgkmcnt(0)" ::: "memory")
#define BARRIER() do { __builtin_amdgcn_s_barrier(); asm volatile("" ::: "memory"); } while (0)
#define SCHED_FENCE() __builtin_amdgcn_sched_barrier(0)
#define LDS_SYNC() do { asm volatile("s_waitcnt lgkmcnt(0)" ::: "memory"); BARRIER(); } while (0)

__device__ __forceinline__ bf8 lds_b128(unsigned a) { return *(LDS const bf8*)(size_t)a; }
__device__ __forceinline__ bf8 lds_tr(unsigned a) {
  bf4 lo = __builtin_amdgcn_ds_read_tr16_b64_v4bf16((LDS bf4*)(size_t)a);
  bf4 hi = __builtin_amdgcn_ds_read_tr16_b64_v4bf16((LDS bf4*)(size_t)(a + 512));
  return __builtin_shufflevector(lo, hi, 0, 1, 2, 3, 4, 5, 6, 7);
}
__device__ __forceinline__ f4 mfma(bf8 a, bf8 b, f4 c) { return __builtin_amdgcn_mfma_f32_16x16x32_bf16(a, b, c, 0, 0, 0); }
__device__ __forceinline__ float bfr(float v) { return (float)(__bf16)v; }
__device__ __forceinline__ float bf2f(u16 v) { return __uint_as_float((unsigned)v << 16); }
__device__ __forceinline__ unsigned pack2(float a, float b) { return __builtin_bit_cast(unsigned, __builtin_convertvector((f2){a, b}, bf2)); }
__device__ __forceinline__ void lds_st(unsigned a, float v) { *(LDS float*)(size_t)a = v; }
__device__ __forceinline__ float lds_ld(unsigned a) { return *(LDS const float*)(size_t)a; }
__device__ __forceinline__ f4 lds_ld4(unsigned a) { return *(LDS const f4*)(size_t)a; }
__device__ __forceinline__ f2 lds_ld2(unsigned a) { return *(LDS const f2*)(size_t)a; }
__device__ __forceinline__ void lds_st2(unsigned a, f2 v) { *(LDS f2*)(size_t)a = v; }
__device__ __forceinline__ void lds_st4(unsigned a, f4 v) { *(LDS f4*)(size_t)a = v; }
__device__ __forceinline__ f2 cmul_add(f2 a, f2 b, f2 c) {
  return (f2){fmaf(-a.y, b.y, fmaf(a.x, b.x, c.x)), fmaf(a.y, b.x, fmaf(a.x, b.y, c.y))};
}
__device__ __forceinline__ f2 cmul(f2 a, f2 b) { return (f2){a.x * b.x - a.y * b.y, a.x * b.y + a.y * b.x}; }

struct Params {
  const u16* x; const u16* lre; const u16* lim; const u16* bre; const u16* bim; const u16* cre; const u16* cim;
  const u16* ldt; const u16* dsk; const u16* w1; const u16* w2; float* out; char* ws;
};

constexpr unsigned C_LAM = 131072;
constexpr unsigned C_MEAN = C_LAM + 1024;
constexpr unsigned C_RINV = C_MEAN + 512;
constexpr unsigned C_CS = C_RINV + 512;
constexpr unsigned C_TAG = C_CS + 1536;
constexpr unsigned C_AGG = C_TAG + 256;
constexpr unsigned C_CIN = C_AGG + 4096;
constexpr unsigned C_JUNK = C_CIN + 4096;
constexpr unsigned C_XT = 98304;
constexpr unsigned LDS_BYTES = C_JUNK + 256;
constexpr int SPIN_MAX = 400000;
constexpr int DRAIN_S = 2;

template <int CTRL> __device__ __forceinline__ float dpp_add(float v) {
  return v + __int_as_float(__builtin_amdgcn_update_dpp(0, __float_as_int(v), CTRL, 0xf, 0xf, true));
}
__device__ __forceinline__ float wave_sum(float v) {
  v = dpp_add<0x111>(v); v = dpp_add<0x112>(v); v = dpp_add<0x114>(v); v = dpp_add<0x118>(v);
  v += __int_as_float(__builtin_amdgcn_update_dpp(0, __float_as_int(v), 0x142, 0xa, 0xf, true));
  v += __int_as_float(__builtin_amdgcn_update_dpp(0, __float_as_int(v), 0x143, 0xc, 0xf, true));
  return __int_as_float(__builtin_amdgcn_readlane(__float_as_int(v), 63));
}
__device__ __forceinline__ void st_gran(char* base, unsigned idx, unsigned tag, unsigned val) {
  __hip_atomic_store((GLB u64*)base + idx, ((u64)tag << 32) | val, __ATOMIC_RELAXED, __HIP_MEMORY_SCOPE_AGENT);
}
__device__ __forceinline__ u64 ld_gran(char* base, unsigned idx) {
  return __hip_atomic_load((GLB u64*)base + idx, __ATOMIC_RELAXED, __HIP_MEMORY_SCOPE_AGENT);
}
__device__ __forceinline__ unsigned wait_gran(char* base, unsigned idx, unsigned tag, u64 v) {
  if (!__all((unsigned)(v >> 32) == tag)) {
    int spin = 0;
    do {
      __builtin_amdgcn_s_sleep(1);
      v = ld_gran(base, idx);
    } while (!__all((unsigned)(v >> 32) == tag) && ++spin < SPIN_MAX);
  }
  return (unsigned)v;
}
__device__ __forceinline__ void st16_dev(char* base, unsigned off, u4 v) {
  asm volatile("global_store_dwordx4 %0, %1, %2 sc1\n\ts_nop 1" :: "v"(off), "v"(v), "s"(base) : "memory");
}
__device__ __forceinline__ unsigned ror8(unsigned v) { return (unsigned)__builtin_amdgcn_update_dpp(0, (int)v, 0x128, 0xf, 0xf, true); }
__device__ __forceinline__ void store_tile(char* ht, unsigned k0, int c, int g, u4 lo, u4 hi) {
  const bool low = c < 8;
  u4 a, b;
#pragma unroll
  for (int i = 0; i < 4; ++i) {
    const unsigned wv = ror8(low ? hi[i] : lo[i]);
    a[i] = low ? lo[i] : wv; b[i] = low ? wv : hi[i];
  }
  const unsigned off = (k0 + (c & 7)) * 128 + (2 * g + (c >> 3)) * 16;
  st16_dev(ht, off, a);
  st16_dev(ht, off + 1024, b);
}
__device__ __forceinline__ void unpack8(u4 v, float* f) {
#pragma unroll
  for (int i = 0; i < 4; ++i) { f[2 * i] = __uint_as_float(v[i] << 16); f[2 * i + 1] = __uint_as_float(v[i] & 0xffff0000u); }
}

template <bool SSM>
__device__ __forceinline__ void body(const Params& p, int rt, int j, int i8) {
  constexpr int NT = SSM ? 2 : 3;
  constexpr int NSLOT = SSM ? 4 : 3;
  constexpr unsigned SLOT = SSM ? 32768 : 40960;
  const int tid = threadIdx.x, lane = tid & 63;
  const int w = __builtin_amdgcn_readfirstlane(tid >> 6);
  const int c = lane & 15, g = lane >> 4;
  const int t0 = rt * 128;
  const int jj = SSM ? j : j + 8;
  const int hf = j >> 2, r4 = rt & 3;
  const int tt = 2 * rt + hf, d0 = i8 * 64;
  int f_lo = 0, f_hi = 0, f_ld = 0;
  if (!SSM) { f_lo = 16 * ((86 * j) >> 3); f_hi = 16 * ((86 * (j + 1)) >> 3); f_ld = f_lo < F - 192 ? f_lo : F - 192; }

  u2 dr = {0u, 0u};
  if (w >= 8) {
    const int L = w - 8;
    unsigned r_lre = 0, r_lim = 0, r_ldt = 0;
    if (SSM && L == 0) {
      const unsigned o = (unsigned)(j * 64 + lane) * 2;
      asm volatile("global_load_ushort %0, %3, %4\n\tglobal_load_ushort %1, %3, %5\n\tglobal_load_ushort %2, %3, %6"
                   : "=&v"(r_lre), "=&v"(r_lim), "=&v"(r_ldt) : "v"(o), "s"(p.lre), "s"(p.lim), "s"(p.ldt));
    }
    unsigned voA[4], voB[6];
#pragma unroll
    for (int u = 0; u < 4; ++u) {
      const int row = 32 * L + 8 * u + (lane >> 3), key = (row & 3) | ((row >> 2) & 4);
      voA[u] = (unsigned)(row * D + (((lane & 7) ^ key) << 3)) * 2 + 4096 - 1024 * u;
    }
    const char* baseA = (const char*)(p.x + (size_t)t0 * D);
    const char* baseB;
    if (SSM) {
#pragma unroll
      for (int u = 0; u < 4; ++u) {
        const int row = (32 * L + 8 * u + (lane >> 3)) & 63;
        voB[u] = (unsigned)(row * D + (((lane & 7) ^ (row & 7)) << 3)) * 2 + 4096 - 1024 * u;
      }
      baseB = (const char*)((L < 2 ? p.bre : p.bim) + (size_t)j * 64 * D);
    } else {
#pragma unroll
      for (int u = 0; u < 6; ++u) {
        const int i = 6 * L + u, k = 8 * (i & 7) + (lane >> 3), key = ((k >> 1) & 1) | ((k >> 2) & 2);
        voB[u] = (unsigned)(k * F + 64 * (i >> 3) + (((lane & 7) ^ (key << 1)) << 3)) * 2 + 4096 - 1024 * (u & 3);
      }
      baseB = (const char*)(p.w1 + f_ld);
    }
    auto stage = [&](int s) {
      const int ks = s;
      const unsigned slot = (unsigned)(s % NSLOT) * SLOT;
      glds_x4(voA, baseA + ks * 128, slot + L * 4096);
      if (SSM) {
        glds_x4(voB, baseB + ks * 128, slot + 16384 + L * 4096);
      } else {
        const char* b = baseB + (size_t)ks * 64 * F * 2;
        glds_x4(voB, b, slot + 16384 + L * 6144);
        glds_x2(voB + 4, b, slot + 16384 + L * 6144 + 4096);
      }
    };
#pragma unroll
    for (int s = 0; s < NSLOT - 1; ++s) stage(s);
    char* const gbase = p.ws + WS_ROW;
    const unsigned gi0 = t0 + 64 * (L & 1) + lane;
    u64 g0 = 0;
    unsigned tag = 0;
#pragma unroll
    for (int s = 0; s < 8; ++s) {
      if (SSM) { if (s < 5) WAIT_VM(16); else if (s == 5) wait_vm<17 + NPF>(); else if (s == 6) wait_vm<9 + NPF>(); else wait_vm<NPF>(); }
      else     { if (s < 6) WAIT_VM(10); else if (s == 6) wait_vm<10 + NPF>(); else wait_vm<NPF>(); }
      BARRIER();
      if (s == 4) asm volatile("global_load_dwordx2 %0, %1, %2 sc1" : "=&v"(g0) : "v"(gi0 * 8), "s"(gbase));
      if (s + NSLOT - 1 < 8) stage(s + NSLOT - 1);
      if (s + NSLOT - 1 == 7) {
        const unsigned sh = r4 * 8 + i8;
        touch((sh * 128 + (L & 1) * 64 + lane) * 128, L < 2 ? p.cre : p.cim, C_JUNK);
        touch((sh * 344 + L * 86 + lane) * 128, p.w2, C_JUNK);
        touch((sh * 344 + L * 86 + 64 + (lane < 22 ? lane : 21)) * 128, p.w2, C_JUNK);
      }
      if (s == 0) {
        tag = *(LDS const unsigned*)(size_t)C_TAG;
        if (SSM && L == 0) {
          asm volatile("" : "+v"(r_lre), "+v"(r_lim), "+v"(r_ldt));
          const float lr = bf2f((u16)r_lre), li = bf2f((u16)r_lim);
          const float dt = bfr(expf(bf2f((u16)r_ldt)));
          const float e = expf(dt * lr);
          float sn, cn; sincosf(dt * li, &sn, &cn);
          const f2 lam = {e * cn, e * sn};
          const float nr = lam.x - 1.0f, ni = lam.y, den = lr * lr + li * li;
          const float inv = 2.0f / den;
          float n0 = fmaf(nr, lr, ni * li), n1 = fmaf(ni, lr, -nr * li);
          asm volatile("" : "+v"(n0), "+v"(n1));
          lds_st2(C_LAM + lane * 8, (f2){lam.x, -lam.y});
          lds_st2(C_LAM + 512 + lane * 8, (f2){n0 * inv, n1 * inv});
        }
      }
      if (s == 7) {
        asm volatile("" : "+v"(g0));
        if (L < 2) {
          const unsigned v0 = wait_gran(gbase, gi0, tag, g0);
          lds_st(C_MEAN + (64 * L + lane) * 4, __uint_as_float(v0 & 0xffff0000u));
          lds_st(C_RINV + (64 * L + lane) * 4, 1.0f / __uint_as_float(v0 << 16));
        }
      }
    }
    LDS_SYNC();

    unsigned voH[4], voN[4], voK[4];
#pragma unroll
    for (int u = 0; u < 4; ++u) {
      const int i = 4 * L + u, r8 = lane >> 3, pos = lane & 7;
      const int k = 8 * i + r8, key = ((k >> 1) & 1) | ((k >> 2) & 2);
      const int chunk = pos ^ (key << 1);
      voH[u] = (unsigned)(k * 128 + chunk * 16) + 4096 - 1024 * u;
      voK[u] = (unsigned)(k * (D * 2) + chunk * 16) + 4096 - 1024 * u;
      const int row = 8 * (i & 7) + r8;
      voN[u] = (unsigned)(row * (P * 2) + (i >> 3) * 128 + ((pos ^ (row & 7)) << 4)) + 4096 - 1024 * u;
    }
    const char* hbase = p.ws + WS_H + (size_t)tt * KH * 128;
    auto stageW = [&](int s) {
      const unsigned slot = (unsigned)(s & 3) * 32768;
      if (s >= 11) {
        const int st = s - 11;
        glds_x4(voN, (const char*)((st < 4 ? p.cre : p.cim) + (size_t)d0 * P + (st & 3) * 128), slot + L * 4096);
      } else {
        const char* wb = (const char*)(p.w2 + (size_t)s * 128 * D + d0);
        if (s == 10 && L == 3) wb -= 32 * D * 2;
        glds_x4(voK, wb, slot + L * 4096);
      }
    };
    auto stageH = [&](int s) {
      const int kb = s >= 11 ? s - 11 : s + 8;
      glds_x4_dev(voH, hbase + (size_t)kb * 16384, (unsigned)(s & 3) * 32768 + 16384 + L * 4096);
    };
    if (!(SSM && L == 0)) { stageW(0); stageW(1); stageW(2); }
    if (SSM) {
      f2 l16 = {0.f, 0.f}, l128 = {0.f, 0.f};
      if (L == 0) {
        l16 = lds_ld2(C_LAM + lane * 8);
#pragma unroll
        for (int i = 0; i < 4; ++i) l16 = cmul(l16, l16);
        l128 = l16;
#pragma unroll
        for (int i = 0; i < 3; ++i) l128 = cmul(l128, l128);
      }
      LDS_SYNC();
      if (L == 0) {
        f2 ag[8];
#pragma unroll
        for (int i = 0; i < 8; ++i) ag[i] = lds_ld2(C_AGG + (i * 64 + lane) * 8);
        f2 tot = ag[0];
#pragma unroll
        for (int i = 1; i < 8; ++i) tot = cmul_add(l16, tot, ag[i]);
        char* rec = p.ws + WS_REC + (size_t)j * 1024;
        if (rt < 15) {
          st_gran(rec, rt * 1024 + lane, tag, __float_as_uint(tot.x));
          st_gran(rec, rt * 1024 + 64 + lane, tag, __float_as_uint(tot.y));
        }
        f2 carry = {0.f, 0.f};
        if (rt > 0) {
          u64 vr[15], vi[15];
          for (int spin = 0; spin < SPIN_MAX; ++spin) {
            bool ok = true;
#pragma unroll
            for (int i = 0; i < 15; ++i) {
              const int ii = i < rt ? i : 0;
              vr[i] = ld_gran(rec, ii * 1024 + lane);
              vi[i] = ld_gran(rec, ii * 1024 + 64 + lane);
            }
#pragma unroll
            for (int i = 0; i < 15; ++i) ok = ok && (unsigned)(vr[i] >> 32) == tag && (unsigned)(vi[i] >> 32) == tag;
            if (__all(ok)) break;
            __builtin_amdgcn_s_sleep(1);
          }
#pragma unroll
          for (int i = 0; i < 15; ++i)
            if (i < rt) carry = cmul_add(l128, carry, (f2){__uint_as_float((unsigned)vr[i]), __uint_as_float((unsigned)vi[i])});
        }
#pragma unroll
        for (int i = 0; i < 8; ++i) {
          lds_st2(C_CIN + (i * 64 + lane) * 8, carry);
          carry = cmul_add(l16, carry, ag[i]);
        }
      }
      LDS_SYNC();
      if (L == 0) { stageW(0); stageW(1); stageW(2); }
    }
    char* const flags = p.ws + WS_FLAG;
    if (!SSM) {
      BARRIER();
      if (L == 0 && lane == 0) st_gran(flags, rt * 16 + jj, tag, 0);
    }
    {
      const unsigned fi = rt * 16 + 8 + (lane & 7);
      u64 v;
      asm volatile("global_load_dwordx2 %0, %1, %2 sc1\n\ts_waitcnt vmcnt(0)" : "=&v"(v) : "v"(fi * 8), "s"(flags) : "memory");
      wait_gran(flags, fi, tag, v);
      asm volatile("s_waitcnt vmcnt(0)" ::: "memory");
    }
    stageH(0); stageH(1); stageH(2);
    u64 fl;
#pragma unroll
    for (int s = 0; s < 19; ++s) {
      if (s == 0) WAIT_VM(8); else if (s == 1) WAIT_VM(12);
      else if (s == 6 || s == 7) WAIT_VM(17);
      else if (s < 17) WAIT_VM(16); else if (s == 17) WAIT_VM(10); else WAIT_VM(2);
      BARRIER();
      if (s == 8) {
        asm volatile("" : "+v"(fl));
        wait_gran(flags, rt * 16 + (lane & 7), tag, fl);
      }
      if (s + 3 < 19) { stageW(s + 3); stageH(s + 3); }
      if (SSM && s == DRAIN_S && L == 0 && lane == 0) st_gran(flags, rt * 16 + jj, tag, 0);
      if (s == 5) asm volatile("global_load_dwordx2 %0, %1, %2 sc1" : "=&v"(fl) : "v"((rt * 16 + (lane & 7)) * 8), "s"(flags));
      if (s == 16) {
        const unsigned r = 16 * L + (lane >> 3), o = r * (D * 2) + (lane & 7) * 16 + 4096;
        const unsigned vo[2] = {o, o + 8 * D * 2 - 1024};
        glds_x2(vo, (const char*)(p.x + (size_t)tt * 64 * D + d0), C_XT + L * 2048);
      }
    }
    if (L == 1 && rt == 0 && jj == 0) {
#pragma unroll
      for (int i = 0; i < 4; ++i) wait_gran(p.ws + WS_ROW, (i * 64 + lane) * 8, tag, ld_gran(p.ws + WS_ROW, (i * 64 + lane) * 8));
      if (lane == 0) __hip_atomic_store((GLB unsigned*)(p.ws + WS_EPOCH), tag, __ATOMIC_RELAXED, __HIP_MEMORY_SCOPE_AGENT);
    }
    WAIT_VM(0);
    LDS_SYNC();
  } else {
    const int m = w >> 2, n = w & 3;
    unsigned rowv;
    {
      u4 xv;
      unsigned ep;
      const unsigned xo = (unsigned)((8 * jj + w) * D + lane * 8) * 2, z = 0;
      asm volatile("global_load_dwordx4 %0, %2, %4\n\tglobal_load_dword %1, %3, %5 sc1\n\ts_waitcnt vmcnt(0)"
                   : "=&v"(xv), "=&v"(ep) : "v"(xo), "v"(z), "s"(p.x + (size_t)t0 * D), "s"(p.ws + WS_EPOCH) : "memory");
      if (w == 0 && lane == 0) *(LDS unsigned*)(size_t)C_TAG = ep + 1u;
      float f[8];
      unpack8(xv, f);
      float sm = 0.f;
#pragma unroll
      for (int i = 0; i < 8; ++i) sm += f[i];
      const float mean = wave_sum(sm) * (1.0f / D);
      float sq = 0.f;
#pragma unroll
      for (int i = 0; i < 8; ++i) { const float dd = f[i] - mean; sq = fmaf(dd, dd, sq); }
      const float var = wave_sum(sq) * (1.0f / D);
      const float mb = bfr(mean), den = bfr(sqrtf(bfr(bfr(var) + 1e-5f)));
      rowv = (__float_as_uint(mb) & 0xffff0000u) | (__float_as_uint(den) >> 16);
      unsigned adA[2], adB[3];
      {
        const int row = 64 * m + 16 * (c >> 2) + (c & 3);
        adA[0] = row * 128 + ((g ^ (c & 7)) << 4);
        adA[1] = adA[0] ^ 64;
        if (SSM) {
          adB[0] = 16384 + (16 * n + c) * 128 + ((g ^ (c & 7)) << 4);
          adB[1] = adB[0] ^ 64;
        } else {
          const int q = c >> 2, pp = c & 3, key = ((q >> 1) & 1) | ((g & 1) << 1);
#pragma unroll
          for (int u = 0; u < 3; ++u) {
            const int ct = 3 * n + u;
            adB[u] = 16384 + (ct >> 2) * 8192 + (8 * g + q) * 128 + (((((ct & 3) ^ key) << 1) | (pp >> 1)) << 4) + 8 * (pp & 1);
          }
        }
      }
      f4 acc[4][NT], asum[NT];
      const __bf16 one = (__bf16)1.0f;
      const bf8 ones = {one, one, one, one, one, one, one, one};
#pragma unroll
      for (int u = 0; u < NT; ++u) asum[u] = (f4){0.f, 0.f, 0.f, 0.f};
#pragma unroll
      for (int a = 0; a < 4; ++a)
#pragma unroll
        for (int u = 0; u < NT; ++u) acc[a][u] = (f4){0.f, 0.f, 0.f, 0.f};
      bf8 A1[4], B1[NT];
      auto frags = [&](unsigned slot, int ks, bf8* A, bf8* B) {
#pragma unroll
        for (int a = 0; a < 4; ++a) A[a] = lds_b128(slot + adA[ks] + 512 * a);
        if (SSM) {
          B[0] = lds_b128(slot + adB[ks]);
          B[1] = lds_b128(slot + adB[ks] + 8192);
        } else {
#pragma unroll
          for (int u = 0; u < 3; ++u) B[u] = lds_tr(slot + adB[u] + ks * 4096);
        }
      };
      auto mm = [&](const bf8* A, const bf8* B) {
#pragma unroll
        for (int a = 0; a < 4; ++a)
#pragma unroll
          for (int u = 0; u < NT; ++u) acc[a][u] = mfma(A[a], B[u], acc[a][u]);
      };
#pragma unroll
      for (int s = 0; s < 8; ++s) {
        LDS_SYNC();
        if (s == 0) {
          const unsigned tag = *(LDS const unsigned*)(size_t)C_TAG;
          if (lane == 0) st_gran(p.ws + WS_ROW, t0 + 8 * jj + w, tag, rowv);
        }
        const unsigned slot = (unsigned)(s % NSLOT) * SLOT;
        bf8 A0[4], B0[NT];
        frags(slot, 0, A0, B0);
        SCHED_FENCE();
        if (s > 0) mm(A1, B1);
        SCHED_FENCE();
#pragma unroll
        for (int u = 0; u < NT; ++u)
          if (s > 0) asum[u] = mfma(ones, m ? B1[u] : B0[u], asum[u]);
          else if (m == 0) asum[u] = mfma(ones, B0[u], asum[u]);
        SCHED_FENCE();
        frags(slot, 1, A1, B1);
        SCHED_FENCE();
        mm(A0, B0);
        SCHED_FENCE();
      }
      mm(A1, B1);
      if (m) {
#pragma unroll
        for (int u = 0; u < NT; ++u) asum[u] = mfma(ones, B1[u], asum[u]);
      }
      if (g == 0) {
#pragma unroll
        for (int u = 0; u < NT; ++u) lds_st(C_CS + (m * 192 + (NT * n + u) * 16 + c) * 4, asum[u][0]);
      }
      LDS_SYNC();
      float ncs[NT];
#pragma unroll
      for (int u = 0; u < NT; ++u)
        ncs[u] = -(lds_ld(C_CS + ((NT * n + u) * 16 + c) * 4) + lds_ld(C_CS + (192 + (NT * n + u) * 16 + c) * 4));
      char* ht = p.ws + WS_H + (size_t)(2 * rt + m) * KH * 128;
      const unsigned amr = (64 * m + 16 * g) * 4;
      if (!SSM) {
        unsigned o[3][8];
#pragma unroll
        for (int a = 0; a < 4; ++a) {
          const f4 mean4 = lds_ld4(C_MEAN + amr + 16 * a), rinv4 = lds_ld4(C_RINV + amr + 16 * a);
          f2 v[6];
#pragma unroll
          for (int q = 0; q < 6; ++q) {
            const int u = q >> 1, i = q & 1;
            v[q] = __builtin_elementwise_fma((f2){mean4[2 * i], mean4[2 * i + 1]}, (f2){ncs[u], ncs[u]}, (f2){acc[a][u][2 * i], acc[a][u][2 * i + 1]});
          }
          SCHED_FENCE();
#pragma unroll
          for (int q = 0; q < 6; ++q) v[q] *= (f2){rinv4[2 * (q & 1)], rinv4[2 * (q & 1) + 1]};
          SCHED_FENCE();
          unsigned pk[6];
#pragma unroll
          for (int q = 0; q < 6; ++q) pk[q] = pack2(v[q].x, v[q].y);
          SCHED_FENCE();
#pragma unroll
          for (int q = 0; q < 6; ++q)
            o[q >> 1][2 * a + (q & 1)] = __builtin_bit_cast(unsigned, __builtin_elementwise_max(__builtin_bit_cast(s2, pk[q]), (s2){0, 0}));
          SCHED_FENCE();
        }
#pragma unroll
        for (int u = 0; u < 3; ++u) {
          const int f0 = f_ld + 16 * (3 * n + u);
          if (f0 >= f_lo && f0 < f_hi)
            store_tile(ht, 1024 + f0, c, g, (u4){o[u][0], o[u][1], o[u][2], o[u][3]}, (u4){o[u][4], o[u][5], o[u][6], o[u][7]});
        }
      } else {
        const int chl = 16 * n + c, seg = 4 * m + g;
        const f2 lam = lds_ld2(C_LAM + chl * 8), cc = lds_ld2(C_LAM + 512 + chl * 8);
        f2 st = {0.f, 0.f};
#pragma unroll
        for (int a = 0; a < 4; ++a) {
          const f4 mean4 = lds_ld4(C_MEAN + amr + 16 * a), rinv4 = lds_ld4(C_RINV + amr + 16 * a);
          f2 vr[2], vi[2], kx[2], ky[2], tr[2], ti[2];
#pragma unroll
          for (int i = 0; i < 2; ++i) {
            const f2 mn = {mean4[2 * i], mean4[2 * i + 1]}, ri = {rinv4[2 * i], rinv4[2 * i + 1]};
            vr[i] = __builtin_elementwise_fma(mn, (f2){ncs[0], ncs[0]}, (f2){acc[a][0][2 * i], acc[a][0][2 * i + 1]});
            vi[i] = __builtin_elementwise_fma(mn, (f2){ncs[1], ncs[1]}, (f2){acc[a][1][2 * i], acc[a][1][2 * i + 1]});
            kx[i] = ri * cc.x; ky[i] = ri * cc.y;
          }
          SCHED_FENCE();
#pragma unroll
          for (int i = 0; i < 2; ++i) { tr[i] = ky[i] * vi[i]; ti[i] = ky[i] * vr[i]; }
          SCHED_FENCE();
#pragma unroll
          for (int i = 0; i < 2; ++i) {
            const f2 br = __builtin_elementwise_fma(kx[i], vr[i], -tr[i]);
            const f2 bi = __builtin_elementwise_fma(-kx[i], vi[i], -ti[i]);
            acc[a][0][2 * i] = br.x; acc[a][0][2 * i + 1] = br.y; acc[a][1][2 * i] = bi.x; acc[a][1][2 * i + 1] = bi.y;
          }
          SCHED_FENCE();
        }
#pragma unroll
        for (int a = 0; a < 4; ++a)
#pragma unroll
          for (int i = 0; i < 4; ++i) st = cmul_add(lam, st, (f2){acc[a][0][i], acc[a][1][i]});
        lds_st2(C_AGG + (seg * 64 + chl) * 8, st);
        LDS_SYNC();
        LDS_SYNC();
        f2 h = lds_ld2(C_CIN + (seg * 64 + chl) * 8);
        u4 ore[2], oim[2];
#pragma unroll
        for (int a = 0; a < 4; ++a) {
          float hr[4], hi[4];
#pragma unroll
          for (int i = 0; i < 4; ++i) {
            h = cmul_add(lam, h, (f2){acc[a][0][i], acc[a][1][i]});
            hr[i] = h.x; hi[i] = h.y;
          }
          ore[a >> 1][2 * (a & 1)] = pack2(hr[0], hr[1]); ore[a >> 1][2 * (a & 1) + 1] = pack2(hr[2], hr[3]);
          oim[a >> 1][2 * (a & 1)] = pack2(hi[0], hi[1]); oim[a >> 1][2 * (a & 1) + 1] = pack2(hi[2], hi[3]);
        }
        store_tile(ht, j * 64 + 16 * n, c, g, ore[0], ore[1]);
        store_tile(ht, 512 + j * 64 + 16 * n, c, g, oim[0], oim[1]);
      }
    }
    if (!SSM) {
      asm volatile("s_waitcnt vmcnt(0)" ::: "memory");
      BARRIER();
    }

    dr = *(const u2*)((const char*)p.dsk + (unsigned)(d0 + 4 * c) * 2);
    const int kq = w & 3, th = w >> 2;
    unsigned adT[4], adN;
    {
      const int q = c >> 2, pp = c & 3, key = ((q >> 1) & 1) | ((g & 1) << 1);
#pragma unroll
      for (int u = 0; u < 4; ++u) adT[u] = (32 * kq + 8 * g + q) * 128 + ((((u ^ key) << 1) | (pp >> 1)) << 4) + 8 * (pp & 1);
      adN = (kq >> 1) * 8192 + c * 128 + (((4 * (kq & 1) + g) ^ (c & 7)) << 4);
    }
    const unsigned adH0 = 16384 + (th ? adT[2] : adT[0]), adH1 = 16384 + (th ? adT[3] : adT[1]);
    f4 ac2[4][2];
#pragma unroll
    for (int a = 0; a < 4; ++a) { ac2[a][0] = (f4){0.f, 0.f, 0.f, 0.f}; ac2[a][1] = (f4){0.f, 0.f, 0.f, 0.f}; }
    for (int s = 0; s < 19; ++s) {
      if (SSM && s == DRAIN_S) asm volatile("s_waitcnt vmcnt(0)" ::: "memory");
      LDS_SYNC();
      const unsigned slot = (unsigned)(s & 3) * 32768;
      if (s == 10 && kq == 3) continue;
      bf8 W[4];
      if (s >= 11) {
#pragma unroll
        for (int a = 0; a < 4; ++a) W[a] = lds_b128(slot + adN + a * 2048);
      } else {
#pragma unroll
        for (int a = 0; a < 4; ++a) W[a] = lds_tr(slot + adT[a]);
      }
      const bf8 H0 = lds_tr(slot + adH0), H1 = lds_tr(slot + adH1);
#pragma unroll
      for (int a = 0; a < 4; ++a) { ac2[a][0] = mfma(W[a], H0, ac2[a][0]); ac2[a][1] = mfma(W[a], H1, ac2[a][1]); }
    }
    LDS_SYNC();
#pragma unroll
    for (int a = 0; a < 4; ++a)
#pragma unroll
      for (int u = 0; u < 2; ++u) {
        const int t = 32 * th + 16 * u + c;
        lds_st4(kq * 16384 + t * 256 + (((4 * a + g) ^ (t & 7)) << 4), ac2[a][u]);
      }
  }
  LDS_SYNC();
  if (w < 8) {
    const f4 dsk = {__uint_as_float(dr.x << 16), __uint_as_float(dr.x & 0xffff0000u), __uint_as_float(dr.y << 16), __uint_as_float(dr.y & 0xffff0000u)};
#pragma unroll
    for (int r = 0; r < 2; ++r) {
      const int t = 8 * w + 4 * r + g;
      const unsigned a = t * 256 + ((c ^ (t & 7)) << 4);
      f4 v = lds_ld4(a) + lds_ld4(a + 16384) + lds_ld4(a + 32768) + lds_ld4(a + 49152);
      const int tl = hf * 64 + t;
      const float mb = lds_ld(C_MEAN + tl * 4), ri = lds_ld(C_RINV + tl * 4);
      const u2 xr = *(LDS const u2*)(size_t)(C_XT + t * 128 + c * 8);
      const float xv[4] = {__uint_as_float(xr.x << 16), __uint_as_float(xr.x & 0xffff0000u),
                           __uint_as_float(xr.y << 16), __uint_as_float(xr.y & 0xffff0000u)};
#pragma unroll
      for (int i = 0; i < 4; ++i) {
        const float xn = bfr(bfr(xv[i] - mb) * ri);
        v[i] = xv[i] + (v[i] + bfr(xn * dsk[i]));
      }
      st16_dev((char*)(p.out + (size_t)tt * 64 * D + d0), (unsigned)(t * D + 4 * c) * 4, __builtin_bit_cast(u4, v));
    }
  }
}

__global__ __launch_bounds__(768, 3) void s5_fused(Params p) {
  const int b = blockIdx.x, q = b & 7, idx = b >> 3;
  const int rt = 4 * (q >> 1) + (idx >> 3), i8 = idx & 7, j = 4 * (q & 1) + (i8 & 3);
  if (i8 < 4) body<true>(p, rt, j, i8);
  else body<false>(p, rt, j, i8);
}

extern "C" void kernel_launch(void* const* d_in, const int* in_sizes, int n_in, void* d_out, int out_size,
                              void* d_ws, size_t ws_size, hipStream_t stream) {
  Params p;
  p.x = (const u16*)d_in[0]; p.lre = (const u16*)d_in[1]; p.lim = (const u16*)d_in[2];
  p.bre = (const u16*)d_in[3]; p.bim = (const u16*)d_in[4]; p.cre = (const u16*)d_in[5]; p.cim = (const u16*)d_in[6];
  p.ldt = (const u16*)d_in[7]; p.dsk = (const u16*)d_in[8]; p.w1 = (const u16*)d_in[10]; p.w2 = (const u16*)d_in[11];
  p.out = (float*)d_out; p.ws = (char*)d_ws;
  (void)hipFuncSetAttribute((const void*)s5_fused, hipFuncAttributeMaxDynamicSharedMemorySize, LDS_BYTES);
  s5_fused<<<256, 768, LDS_BYTES, stream>>>(p);
}
```
